```python
import torch

def _softmax(x, axis=-1):
    return torch.softmax(x, dim=axis)
def _gelu(x):
    return torch.nn.functional.gelu(x, approximate="tanh")
def _silu(x):
    return torch.nn.functional.silu(x)
def _sigmoid(x):
    return torch.sigmoid(x)
def _layernorm(x, axis=-1, eps=1e-5):
    m = x.mean(dim=axis, keepdim=True)
    v = x.var(dim=axis, keepdim=True, unbiased=False)
    return (x - m) / torch.sqrt(v + eps)
def _scatter_add(tgt, idx, src, axis=0):
    return tgt.clone().index_add_(axis, idx, src)
def _segment_sum(src, seg, n):
    out = torch.zeros((n,) + src.shape[1:], dtype=src.dtype)
    return out.index_add_(0, seg, src)
def _pad(x, pw):
    flat = tuple(v for pair in reversed(pw) for v in pair)
    return torch.nn.functional.pad(x, flat)

kernel_name = 'parallel_sum__linear_attn_block__mixer_channel_mlp__prompt'

def setup_inputs():

    _w = (lambda a: (lambda t: t.to(torch.bfloat16) if t.is_floating_point() else t)(torch.as_tensor(a)))
    def _A_setup():
        B, S, D = 1, 2048, 640
        return {
            "x": _w((lambda *s: torch.randn(*s, dtype=torch.bfloat16) * 0.05)(B, S, D)),
            "wqkv": _w((lambda *s: torch.randn(*s, dtype=torch.bfloat16) * 0.1)(D, 3 * D)),
            "wo": _w((lambda *s: torch.randn(*s, dtype=torch.bfloat16) * 0.05)(D, D)),
        }
    _A_in = _A_setup()

    def _B_setup():
        B, S, D, F = 1, 2048, 640, 1536
        return {
            "x": _w((lambda *s: torch.randn(*s, dtype=torch.bfloat16) * 0.05)(B, S, D)),
            "w1": _w((lambda *s: torch.randn(*s, dtype=torch.bfloat16) * 1.6)(D, F) * (D ** -0.5)),
            "b1": _w((lambda *s: torch.randn(*s, dtype=torch.bfloat16) * 0.8)(F)),
            "w2": _w((lambda *s: torch.randn(*s, dtype=torch.bfloat16) * 0.05)(F, D) * (F ** -0.5)),
            "b2": _w((lambda *s: torch.randn(*s, dtype=torch.bfloat16) * 0.05)(D)),
        }
    _B_in = _B_setup()

    return {
        "A_x": _A_in["x"],
        "A_wqkv": _A_in["wqkv"],
        "A_wo": _A_in["wo"],
        "B_x": _B_in["x"],
        "B_w1": _B_in["w1"],
        "B_b1": _B_in["b1"],
        "B_w2": _B_in["w2"],
        "B_b2": _B_in["b2"],
    }

def reference(A_x, A_wqkv, A_wo, B_x, B_w1, B_b1, B_w2, B_b2):
    def _A_ref(x, wqkv, wo):
        B, S, D = x.shape
        H = 32; Dh = D // H
        qkv = (lambda x, *d: x.permute(*d))((x @ wqkv).reshape(B, S, 3, H, Dh), 2, 0, 3, 1, 4)
        q, k, v = qkv[0], qkv[1], qkv[2]
        phi = lambda t: torch.where(t > 0, t + 1, torch.exp(t)) + 1e-6
        pq, pk = phi(q), phi(k)
        kv = torch.einsum("bhsd,bhse->bhde", pk, v)
        z = torch.einsum("bhsd->bhd", pk)
        num = torch.einsum("bhqd,bhde->bhqe", pq, kv)
        den = torch.einsum("bhqd,bhd->bhq", pq, z)[..., None] + 1e-6
        o = num / den
        return (lambda x, *d: x.permute(*d))(o, 0, 2, 1, 3).reshape(B, S, D) @ wo

    def _B_ref(x, w1, b1, w2, b2):
        y = _layernorm(x, axis=-1)
        h = _gelu(torch.einsum("bsd,df->bsf", y, w1) + b1)
        return torch.einsum("bsf,fd->bsd", h, w2) + b2

    x = A_x
    xn = _layernorm(x, axis=-1)
    return x + _A_ref(xn, A_wqkv, A_wo) + _B_ref(xn, B_w1, B_b1, B_w2, B_b2)
```

```cpp
#include <hip/hip_runtime.h>
#include <hip/hip_bf16.h>
#include <stdint.h>

typedef __attribute__((ext_vector_type(8))) __bf16 bf16x8;
typedef __attribute__((ext_vector_type(4))) __bf16 bf16x4;
typedef __attribute__((ext_vector_type(2))) __bf16 bf16x2;
typedef __attribute__((ext_vector_type(4))) float f32x4;
typedef __attribute__((ext_vector_type(2))) float f32x2;
typedef __attribute__((ext_vector_type(4))) unsigned u32x4;
typedef __attribute__((ext_vector_type(2))) unsigned u32x2;
#define LAS __attribute__((address_space(3)))
#define GAS __attribute__((address_space(1)))
typedef GAS unsigned gu32;
typedef GAS unsigned long long gu64;

namespace {

constexpr int S_ = 2048, D_ = 640, F_ = 1536;
constexpr int NU = 68;
constexpr size_t OFF_AF = 0;
constexpr size_t OFF_BF = OFF_AF + (size_t)128 * NU * 1024;
constexpr size_t OFF_KVP = OFF_BF + (size_t)40 * NU * 1024;
constexpr size_t OFF_ZP = OFF_KVP + (size_t)16 * 32 * 400 * 4;
constexpr size_t OFF_FLAG = OFF_ZP + (size_t)16 * 640 * 4;
constexpr size_t WS_BYTES = OFF_FLAG + 8 * 32 * 4;

constexpr int K1_XB = 16384;
constexpr int K1_STAGE = K1_XB + 30720;
constexpr int K1_LDS = 3 * K1_STAGE;

__device__ __forceinline__ u32x4 make_rsrc(const void* p, unsigned bytes) {
  const unsigned long long a = (unsigned long long)p;
  u32x4 r;
  r[0] = (unsigned)a;
  r[1] = (unsigned)(a >> 32) & 0xffffu;
  r[2] = bytes;
  r[3] = 0x00020000u;
  return r;
}

__device__ __forceinline__ void dma16(u32x4 rsrc, unsigned voff, unsigned soff, unsigned lds) {
  unsigned keep;
  asm volatile(
      "s_mov_b32 %0, m0\n\t"
      "s_mov_b32 m0, %4\n\t"
      "s_nop 0\n\t"
      "buffer_load_dwordx4 %1, %2, %3 offen lds\n\t"
      "s_mov_b32 m0, %0"
      : "=&s"(keep)
      : "v"(voff), "s"(rsrc), "s"(soff), "s"(lds)
      : "memory");
}

__device__ __forceinline__ unsigned pack2(float a, float b) {
  f32x2 p = {a, b};
  return __builtin_bit_cast(unsigned, __builtin_convertvector(p, bf16x2));
}
__device__ __forceinline__ float bflo(unsigned u) { return __builtin_bit_cast(float, u << 16); }
__device__ __forceinline__ float bfhi(unsigned u) { return __builtin_bit_cast(float, u & 0xffff0000u); }
__device__ __forceinline__ float dot2(unsigned a, unsigned b, float c) {
  return __builtin_amdgcn_fdot2_f32_bf16(__builtin_bit_cast(bf16x2, a), __builtin_bit_cast(bf16x2, b), c, false);
}
__device__ __forceinline__ f32x4 mfma16(bf16x8 a, bf16x8 b, f32x4 c) {
  return __builtin_amdgcn_mfma_f32_16x16x32_bf16(a, b, c, 0, 0, 0);
}
__device__ __forceinline__ bf16x8 tr_frag(const LAS char* p0, const LAS char* p1) {
  bf16x4 lo = __builtin_amdgcn_ds_read_tr16_b64_v4bf16((LAS bf16x4*)p0);
  bf16x4 hi = __builtin_amdgcn_ds_read_tr16_b64_v4bf16((LAS bf16x4*)p1);
  return __builtin_shufflevector(lo, hi, 0, 1, 2, 3, 4, 5, 6, 7);
}
__device__ __forceinline__ float phi(float t) {
  const float e = __builtin_amdgcn_exp2f(t * 1.4426950408889634f);
  return (t > 0.f ? t + 1.f : e) + 1e-6f;
}
__device__ __forceinline__ float gelu_tanh(float x) {
  const float c1 = -2.f * 0.7978845608028654f * 1.4426950408889634f;
  const float c2 = c1 * 0.044715f;
  const float t = x * __builtin_fmaf(x * x, c2, c1);
  const float e = __builtin_amdgcn_exp2f(t);
  return x * __builtin_amdgcn_rcpf(1.f + e);
}

template <bool QKV>
__device__ __forceinline__ void k1_body(const __hip_bfloat16* __restrict__ x, const __hip_bfloat16* __restrict__ w,
                                        const __hip_bfloat16* __restrict__ b1, const __hip_bfloat16* __restrict__ w2,
                                        char* __restrict__ ws, int mtile, int grp, LAS char* smem) {
  constexpr int CPR = QKV ? 30 : 24;
  constexpr int RS = CPR * 16;
  constexpr int LDW = QKV ? 1920 : 1536;
  constexpr int NWS = QKV ? 4 : 3;
  constexpr int NTW = QKV ? 4 : 3;
  constexpr int NDMA = NWS + 2;

  const int tid = threadIdx.x, lane = tid & 63;
  const int wid = __builtin_amdgcn_readfirstlane(tid >> 6);
  const int mw = wid >> 2, nw = wid & 3;
  const int l15 = lane & 15, g = lane >> 4;
  const unsigned lds0 = (unsigned)(size_t)smem;

  const u32x4 rx = make_rsrc(x, S_ * D_ * 2);
  const u32x4 rw = make_rsrc(w, D_ * LDW * 2);

  unsigned xoff[2], woff[NWS];
#pragma unroll
  for (int j = 0; j < 2; ++j) {
    const int piece = 2 * wid + j, row = piece * 8 + (lane >> 3), ch = (lane & 7) ^ (row & 7);
    xoff[j] = (unsigned)(((mtile * 128 + row) * D_ + ch * 8) * 2);
  }
#pragma unroll
  for (int j = 0; j < NWS; ++j) {
    int piece = wid + 8 * j;
    if (QKV && piece > 29) piece = 29;
    const int ci = piece * 64 + lane, rho = ci / CPR, cp = ci - rho * CPR;
    const int kl = (rho & ~12) | ((rho & 4) << 1) | ((rho & 8) >> 1);
    int col;
    if (QKV) {
      const int part = cp / 10, wi = cp - part * 10;
      col = part * 640 + grp * 80 + wi * 8;
    } else {
      const int c = cp ^ (2 * ((rho >> 1) & 3));
      col = grp * 192 + c * 8;
    }
    woff[j] = (unsigned)((kl * LDW + col) * 2);
  }
  auto issue = [&](int s, int buf) {
    const unsigned base = lds0 + buf * K1_STAGE;
#pragma unroll
    for (int j = 0; j < 2; ++j) dma16(rx, xoff[j], (unsigned)(s * 128), base + (2 * wid + j) * 1024);
#pragma unroll
    for (int j = 0; j < NWS; ++j) {
      int piece = wid + 8 * j;
      if (QKV && piece > 29) piece = 29;
      dma16(rw, woff[j], (unsigned)(s * 64 * LDW * 2), base + K1_XB + piece * 1024);
    }
  };
  issue(0, 0);
  issue(1, 1);

  unsigned w2r[8];
  const int job = (mtile * 8 + grp) * 8 + wid;
  if (!QKV && job < 960) {
    const int u = job / 20, nb = job - u * 20;
#pragma unroll
    for (int j = 0; j < 8; ++j) {
      const int k = 32 * u + 16 * (j >> 2) + 4 * g + (j & 3);
      w2r[j] = *(const unsigned*)(w2 + (size_t)k * D_ + nb * 32 + 2 * l15);
    }
  }

  int xrd[4];
#pragma unroll
  for (int i = 0; i < 4; ++i) xrd[i] = (mw * 64 + ((i + nw) & 3) * 16 + l15) * 128;
  const int xsw0 = ((g) ^ (lane & 7)) << 4, xsw1 = ((4 + g) ^ (lane & 7)) << 4;
  const int ga = g >> 1, gb = g & 1, dq = (lane >> 2) & 3, p = lane & 3;
  const int rho0 = 16 * ga + 4 * gb + dq;
  int wrd[NTW];
#pragma unroll
  for (int t = 0; t < NTW; ++t) {
    int T = nw * NTW + ((t < 2 || NTW == 4) ? (t ^ mw) : t);
    if (QKV && T > 14) T = 14;
    int ch = 2 * T + (p >> 1);
    if (!QKV) ch ^= 2 * ((rho0 >> 1) & 3);
    wrd[t] = K1_XB + rho0 * RS + ch * 16 + (p & 1) * 8;
  }

  f32x4 acc[4][NTW];
#pragma unroll
  for (int i = 0; i < 4; ++i)
#pragma unroll
    for (int t = 0; t < NTW; ++t) acc[i][t] = (f32x4){0.f, 0.f, 0.f, 0.f};
  float rsum = 0.f, rsq = 0.f;
  float csum[2] = {0.f, 0.f};
  const unsigned ones = 0x3f803f80u;

#pragma unroll
  for (int s = 0; s < 10; ++s) {
    if (s < 9)
      asm volatile("s_waitcnt vmcnt(%0) lgkmcnt(0)\n\ts_barrier" ::"n"(NDMA) : "memory");
    else
      asm volatile("s_waitcnt vmcnt(0) lgkmcnt(0)\n\ts_barrier" ::: "memory");
    if (s + 2 < 10) issue(s + 2, (s + 2) % 3);
    const LAS char* sb = smem + (s % 3) * K1_STAGE;
#pragma unroll
    for (int kk = 0; kk < 2; ++kk) {
      bf16x8 xf[4], wf[NTW];
#pragma unroll
      for (int i = 0; i < 4; ++i) xf[i] = *(const LAS bf16x8*)(sb + xrd[i] + (kk ? xsw1 : xsw0));
#pragma unroll
      for (int t = 0; t < NTW; ++t) wf[t] = tr_frag(sb + wrd[t] + kk * 32 * RS, sb + wrd[t] + kk * 32 * RS + 8 * RS);
#pragma unroll
      for (int t = 0; t < NTW; ++t) {
#pragma unroll
        for (int i = 0; i < 4; ++i) acc[i][t] = mfma16(wf[t], xf[i], acc[i][t]);
      }
      {
        const u32x4 xs = __builtin_bit_cast(u32x4, xf[0]);
        const u32x4 wa = __builtin_bit_cast(u32x4, wf[0]);
        const u32x4 wb = __builtin_bit_cast(u32x4, wf[2]);
#pragma unroll
        for (int j = 0; j < 4; ++j) {
          rsum = dot2(xs[j], ones, rsum);
          rsq = dot2(xs[j], xs[j], rsq);
          csum[0] = dot2(wa[j], ones, csum[0]);
          csum[1] = dot2(wb[j], ones, csum[1]);
        }
        asm volatile("" : "+v"(rsum), "+v"(rsq), "+v"(csum[0]), "+v"(csum[1]));
      }
    }
  }

  LAS float* st_mean = (LAS float*)(smem + 98304);
  LAS float* st_rstd = st_mean + 128;
  LAS float* st_cs = st_mean + 256;
  rsum += __shfl_xor(rsum, 16);
  rsum += __shfl_xor(rsum, 32);
  rsq += __shfl_xor(rsq, 16);
  rsq += __shfl_xor(rsq, 32);
#pragma unroll
  for (int j = 0; j < 2; ++j) {
    csum[j] += __shfl_xor(csum[j], 16);
    csum[j] += __shfl_xor(csum[j], 32);
  }
  asm volatile("s_waitcnt lgkmcnt(0)\n\ts_barrier" ::: "memory");
  if (g == 0) {
    const float mean = rsum * (1.f / 640.f);
    float var = rsq * (1.f / 640.f) - mean * mean;
    var = var > 0.f ? var : 0.f;
    float r = __builtin_amdgcn_rsqf(var + 1e-5f);
    if (!QKV) r *= __builtin_amdgcn_rsqf(var * r * r + 1e-5f);
    st_mean[(4 * mw + nw) * 16 + l15] = mean;
    st_rstd[(4 * mw + nw) * 16 + l15] = r;
    st_cs[(nw * NTW + mw) * 16 + l15] = csum[0];
    if (mw == 0 || NTW == 4) st_cs[(nw * NTW + (2 ^ mw)) * 16 + l15] = csum[1];
  }
  asm volatile("s_waitcnt lgkmcnt(0)\n\ts_barrier" ::: "memory");
  float mean[4], rstd[4];
#pragma unroll
  for (int i = 0; i < 4; ++i) {
    mean[i] = st_mean[(4 * mw + ((i + nw) & 3)) * 16 + l15];
    rstd[i] = st_rstd[(4 * mw + ((i + nw) & 3)) * 16 + l15];
  }
  char* af = ws + OFF_AF;

  if (!QKV) {
#pragma unroll
    for (int t = 0; t < NTW; ++t) {
      const int T = nw * NTW + (t < 2 ? (t ^ mw) : t);
      const int Tg = grp * 12 + T;
      const f32x4 cs = *(const LAS f32x4*)(st_cs + T * 16 + 4 * g);
      const u32x2 bb = *(const u32x2*)(b1 + Tg * 16 + 4 * g);
      const float bv[4] = {bflo(bb[0]), bfhi(bb[0]), bflo(bb[1]), bfhi(bb[1])};
#pragma unroll
      for (int i = 0; i < 4; ++i) {
        float hv[4];
        const float mr = -mean[i] * rstd[i];
#pragma unroll
        for (int r = 0; r < 4; ++r) hv[r] = gelu_tanh(__builtin_fmaf(acc[i][t][r], rstd[i], __builtin_fmaf(mr, cs[r], bv[r])));
        const int m16 = mtile * 8 + mw * 4 + ((i + nw) & 3);
        u32x2 o = {pack2(hv[0], hv[1]), pack2(hv[2], hv[3])};
        *(u32x2*)(af + ((size_t)(m16 * NU + (Tg >> 1)) * 64 + lane) * 16 + (Tg & 1) * 8) = o;
      }
    }
    if (job < 960) {
      const int u = job / 20, nb = job - u * 20;
      u32x4 lo, hi;
#pragma unroll
      for (int j = 0; j < 4; ++j) {
        lo[j] = __builtin_amdgcn_perm(w2r[2 * j + 1], w2r[2 * j], 0x05040100u);
        hi[j] = __builtin_amdgcn_perm(w2r[2 * j + 1], w2r[2 * j], 0x07060302u);
      }
      const int n16 = nb * 2 + (l15 >> 3), nl = (2 * l15) & 15;
      char* dst = ws + OFF_BF + ((size_t)(n16 * NU + u) * 64 + g * 16 + nl) * 16;
      *(u32x4*)dst = lo;
      *(u32x4*)(dst + 16) = hi;
    }
  } else {
    constexpr int PVS = 352;
#pragma unroll
    for (int t = 0; t < NTW; ++t) {
      const int T = nw * NTW + (t ^ mw);
      if (T > 14) continue;
      const f32x4 cs = *(const LAS f32x4*)(st_cs + T * 16 + 4 * g);
#pragma unroll
      for (int i = 0; i < 4; ++i) {
        float v[4];
        const float mr = -mean[i] * rstd[i];
#pragma unroll
        for (int r = 0; r < 4; ++r) {
          v[r] = __builtin_fmaf(acc[i][t][r], rstd[i], mr * cs[r]);
          if (T < 10) v[r] = phi(v[r]);
        }
        u32x2 o = {pack2(v[0], v[1]), pack2(v[2], v[3])};
        if (T < 5) {
          const int m16 = mtile * 8 + mw * 4 + ((i + nw) & 3), Tg = grp * 5 + T;
          *(u32x2*)(af + ((size_t)(m16 * NU + 48 + (Tg >> 1)) * 64 + lane) * 16 + (Tg & 1) * 8) = o;
        } else {
          *(LAS u32x2*)(smem + (mw * 64 + ((i + nw) & 3) * 16 + l15) * PVS + (T - 5) * 32 + g * 8) = o;
        }
      }
    }
    asm volatile("s_waitcnt lgkmcnt(0)\n\ts_barrier" ::: "memory");
    if (wid < 5) {
      const int dt = wid;
      const int trow = (4 * g + dq) * PVS + (p >> 1) * 16 + (p & 1) * 8;
      f32x4 kvacc[3], zacc = {0.f, 0.f, 0.f, 0.f};
#pragma unroll
      for (int j = 0; j < 3; ++j) kvacc[j] = (f32x4){0.f, 0.f, 0.f, 0.f};
      const u32x4 onesv = {ones, ones, ones, ones};
#pragma unroll
      for (int ks = 0; ks < 4; ++ks) {
        const LAS char* pb = smem + ks * 32 * PVS + trow;
        const bf16x8 a = tr_frag(pb + dt * 32, pb + dt * 32 + 16 * PVS);
        zacc = mfma16(a, __builtin_bit_cast(bf16x8, onesv), zacc);
#pragma unroll
        for (int j = 0; j < 3; ++j) {
          int et = dt - 1 + j;
          et = et < 0 ? 0 : (et > 4 ? 4 : et);
          const bf16x8 b = tr_frag(pb + 160 + et * 32, pb + 160 + et * 32 + 16 * PVS);
          kvacc[j] = mfma16(a, b, kvacc[j]);
        }
      }
      float* kvp = (float*)(ws + OFF_KVP) + (size_t)(mtile * 32 + grp * 4) * 400;
#pragma unroll
      for (int j = 0; j < 3; ++j) {
        const int et = dt - 1 + j;
        if (et < 0 || et > 4) continue;
        const int e = et * 16 + l15, he = e / 20;
#pragma unroll
        for (int r = 0; r < 4; ++r) {
          const int d = dt * 16 + 4 * g + r, hd = d / 20;
          if (hd == he) kvp[hd * 400 + (d - hd * 20) * 20 + (e - he * 20)] = kvacc[j][r];
        }
      }
      if (l15 == 0) *(f32x4*)((float*)(ws + OFF_ZP) + mtile * 640 + grp * 80 + dt * 16 + 4 * g) = zacc;
    }
  }
}

__global__ void __launch_bounds__(512, 2)
k1_kernel(const __hip_bfloat16* __restrict__ x, const __hip_bfloat16* __restrict__ wqkv,
          const __hip_bfloat16* __restrict__ w1, const __hip_bfloat16* __restrict__ b1,
          const __hip_bfloat16* __restrict__ w2, char* __restrict__ ws) {
  extern __shared__ __attribute__((aligned(16))) char smem_[];
  LAS char* smem = (LAS char*)smem_;
  const int b = blockIdx.x, xcd = b & 7, idx = b >> 3;
  const int mtile = (xcd & 1) * 8 + (idx & 7), sel = idx >> 3;
  const int grp = (xcd >> 1) * 2 + (sel & 1);
  if (b == 0 && threadIdx.x < 256) ((unsigned*)(ws + OFF_FLAG))[threadIdx.x] = 0u;
  if (sel >> 1)
    k1_body<false>(x, w1, b1, w2, ws, mtile, grp, smem);
  else
    k1_body<true>(x, wqkv, b1, w2, ws, mtile, grp, smem);
}

constexpr int K2_LDS = 61440 + 2048;

__global__ void __launch_bounds__(256, 1)
k2_kernel(const __hip_bfloat16* __restrict__ x, const __hip_bfloat16* __restrict__ wo,
          const __hip_bfloat16* __restrict__ b2, char* __restrict__ ws, __hip_bfloat16* __restrict__ out) {
  extern __shared__ __attribute__((aligned(16))) char smem_[];
  LAS char* smem = (LAS char*)smem_;
  const int tid = threadIdx.x, lane = tid & 63;
  const int wid = __builtin_amdgcn_readfirstlane(tid >> 6);
  const int l15 = lane & 15, g = lane >> 4;
  const int b = blockIdx.x, xcd = b & 7, idx = b >> 3;
  const int mt = (xcd >> 1) * 8 + (idx & 7);
  const int nt = (xcd & 1) * 4 + (idx >> 3);
  const int ph = b >> 3, ps = b & 7;

  const __amdgpu_buffer_rsrc_t ra = __builtin_amdgcn_make_buffer_rsrc(ws + OFF_AF, 0, 128 * NU * 1024, 0x00020000);
  const __amdgpu_buffer_rsrc_t rb = __builtin_amdgcn_make_buffer_rsrc(ws + OFF_BF, 0, 40 * NU * 1024, 0x00020000);

  LAS float* kvs = (LAS float*)(smem + 61440);
  LAS unsigned* zs = (LAS unsigned*)(smem + 61440 + 1600) ;
  (void)zs;
  LAS char* zbuf = smem + wid * 320;

  f32x2 kvsum = {0.f, 0.f};
  if (tid < 200) {
    const f32x2* src = (const f32x2*)(ws + OFF_KVP) + ph * 200 + tid;
#pragma unroll
    for (int pidx = 0; pidx < 16; ++pidx) kvsum += src[(size_t)pidx * 32 * 200];
  }
  const int pg = tid / 40, pn = tid - pg * 40;
  unsigned wor[20];
  if (tid < 200) {
#pragma unroll
    for (int e = 0; e < 20; ++e) wor[e] = *(const unsigned*)(wo + (size_t)(ph * 20 + e) * D_ + ps * 80 + 2 * pn);
  }
  f32x4 zsum = {0.f, 0.f, 0.f, 0.f};
  if (lane < 40) {
    const f32x4* src = (const f32x4*)(ws + OFF_ZP) + wid * 40 + lane;
#pragma unroll
    for (int pidx = 0; pidx < 16; ++pidx) zsum += src[pidx * 160];
  }

  const unsigned aoff = (unsigned)(mt * 4 * NU * 1024 + lane * 16);
  const unsigned boff = (unsigned)(nt * 5 * NU * 1024 + lane * 16);
  u32x4 fa[3][4], fb[3][5];
  auto load_stage = [&](int u, int slot) {
#pragma unroll
    for (int i = 0; i < 4; ++i) fa[slot][i] = __builtin_amdgcn_raw_buffer_load_b128(ra, aoff + i * NU * 1024, u * 1024, 0);
#pragma unroll
    for (int j = 0; j < 5; ++j) fb[slot][j] = __builtin_amdgcn_raw_buffer_load_b128(rb, boff + j * NU * 1024, u * 1024, 0);
  };
  const int u0 = wid * 12;
  load_stage(u0 + 0, 0);
  load_stage(u0 + 1, 1);
  load_stage(u0 + 2, 2);
  u32x4 pq[5][4];
#pragma unroll
  for (int ul = 0; ul < 5; ++ul)
#pragma unroll
    for (int i = 0; i < 4; ++i)
      pq[ul][i] = __builtin_amdgcn_raw_buffer_load_b128(ra, aoff + i * NU * 1024, (48 + wid * 5 + ul) * 1024, 0);

  if (tid < 200) {
    const unsigned pk = pack2(kvsum[0], kvsum[1]);
    *(LAS f32x2*)(kvs + 2 * tid) = (f32x2){bflo(pk), bfhi(pk)};
  }
  if (lane < 40) *(LAS u32x2*)(zbuf + lane * 8) = (u32x2){pack2(zsum[0], zsum[1]), pack2(zsum[2], zsum[3])};
  __syncthreads();
  if (tid < 200) {
    float o0[4] = {0.f, 0.f, 0.f, 0.f}, o1[4] = {0.f, 0.f, 0.f, 0.f};
#pragma unroll
    for (int r = 0; r < 4; ++r) {
#pragma unroll
      for (int e4 = 0; e4 < 5; ++e4) {
        const f32x4 kq = *(const LAS f32x4*)(kvs + (pg * 4 + r) * 20 + e4 * 4);
#pragma unroll
        for (int c = 0; c < 4; ++c) {
          o0[r] = __builtin_fmaf(kq[c], bflo(wor[e4 * 4 + c]), o0[r]);
          o1[r] = __builtin_fmaf(kq[c], bfhi(wor[e4 * 4 + c]), o1[r]);
        }
      }
    }
    const int q = 5 * ph + pg, n = ps * 80 + 2 * pn;
    char* dst = ws + OFF_BF + ((size_t)((n >> 4) * NU + 48 + (q >> 3)) * 64 + (q & 3) * 16 + (n & 15)) * 16 + ((q >> 2) & 1) * 8;
    const unsigned long long v0 = (unsigned long long)pack2(o0[0], o0[1]) | ((unsigned long long)pack2(o0[2], o0[3]) << 32);
    const unsigned long long v1 = (unsigned long long)pack2(o1[0], o1[1]) | ((unsigned long long)pack2(o1[2], o1[3]) << 32);
    __hip_atomic_store((gu64*)dst, v0, __ATOMIC_RELAXED, __HIP_MEMORY_SCOPE_AGENT);
    __hip_atomic_store((gu64*)(dst + 16), v1, __ATOMIC_RELAXED, __HIP_MEMORY_SCOPE_AGENT);
  }
  asm volatile("s_waitcnt vmcnt(0)" ::: "memory");
  __syncthreads();
  if (tid == 0)
    __hip_atomic_store((gu32*)(ws + OFF_FLAG) + ps * 32 + ph, 1u, __ATOMIC_RELAXED, __HIP_MEMORY_SCOPE_AGENT);

  {
    u32x2 zp[5][2];
#pragma unroll
    for (int ul = 0; ul < 5; ++ul)
#pragma unroll
      for (int i = 0; i < 2; ++i) zp[ul][i] = *(const LAS u32x2*)(zbuf + (32 * ul + 16 * i + 4 * g) * 2);
    float rden[4][12];
#pragma unroll
    for (int c = 0; c < 3; ++c) {
      const int htar = (16 * (4 * c + (l15 & 3)) + 4 * (l15 >> 2)) / 20;
      f32x4 dacc[4];
#pragma unroll
      for (int i = 0; i < 4; ++i) dacc[i] = (f32x4){0.f, 0.f, 0.f, 0.f};
#pragma unroll
      for (int ul = 0; ul < 5; ++ul) {
        if (c == 0 && ul > 2) continue;
        if (c == 1 && ul < 1) continue;
        if (c == 2 && ul < 3) continue;
        u32x4 zf;
#pragma unroll
        for (int i = 0; i < 2; ++i) {
          const bool m = (32 * ul + 16 * i + 4 * g) / 20 == htar;
          zf[2 * i] = m ? zp[ul][i][0] : 0u;
          zf[2 * i + 1] = m ? zp[ul][i][1] : 0u;
        }
#pragma unroll
        for (int i = 0; i < 4; ++i)
          dacc[i] = mfma16(__builtin_bit_cast(bf16x8, zf), __builtin_bit_cast(bf16x8, pq[ul][i]), dacc[i]);
      }
#pragma unroll
      for (int i = 0; i < 4; ++i)
#pragma unroll
        for (int r = 0; r < 4; ++r) rden[i][4 * c + r] = __builtin_amdgcn_rcpf(dacc[i][r] + 1e-6f);
    }
#pragma unroll
    for (int ul = 0; ul < 5; ++ul)
#pragma unroll
      for (int i = 0; i < 4; ++i)
#pragma unroll
        for (int j = 0; j < 4; ++j) {
          const float sc = rden[i][2 * ul + (j >> 1)];
          pq[ul][i][j] = pack2(bflo(pq[ul][i][j]) * sc, bfhi(pq[ul][i][j]) * sc);
        }
  }

  f32x4 acc[4][5];
#pragma unroll
  for (int i = 0; i < 4; ++i)
#pragma unroll
    for (int j = 0; j < 5; ++j) acc[i][j] = (f32x4){0.f, 0.f, 0.f, 0.f};

#pragma unroll
  for (int s = 0; s < 12; ++s) {
    const int slot = s % 3;
#pragma unroll
    for (int i = 0; i < 4; ++i)
#pragma unroll
      for (int j = 0; j < 5; ++j)
        acc[i][j] = mfma16(__builtin_bit_cast(bf16x8, fb[slot][j]), __builtin_bit_cast(bf16x8, fa[slot][i]), acc[i][j]);
    if (s + 3 < 12) load_stage(u0 + s + 3, slot);
  }

  {
    gu32* fl = (gu32*)(ws + OFF_FLAG) + nt * 32 + wid * 8 + (lane & 7);
    for (unsigned spins = 0; spins < (1u << 22); ++spins) {
      const unsigned v = __hip_atomic_load(fl, __ATOMIC_RELAXED, __HIP_MEMORY_SCOPE_AGENT);
      if (__all(v == 1u)) break;
      __builtin_amdgcn_s_sleep(1);
    }
    __builtin_amdgcn_fence(__ATOMIC_ACQUIRE, "wavefront");
  }
  {
    u32x4 wf[5][5];
#pragma unroll
    for (int ul = 0; ul < 5; ++ul)
#pragma unroll
      for (int j = 0; j < 5; ++j)
        wf[ul][j] = __builtin_amdgcn_raw_buffer_load_b128(rb, boff + j * NU * 1024, (48 + wid * 5 + ul) * 1024, 16);
#pragma unroll
    for (int ul = 0; ul < 5; ++ul)
#pragma unroll
      for (int i = 0; i < 4; ++i)
#pragma unroll
        for (int j = 0; j < 5; ++j)
          acc[i][j] = mfma16(__builtin_bit_cast(bf16x8, wf[ul][j]), __builtin_bit_cast(bf16x8, pq[ul][i]), acc[i][j]);
  }

  const int token = mt * 64 + wid * 16 + l15;
  u32x2 xr[5], br[5];
#pragma unroll
  for (int j = 0; j < 5; ++j) {
    xr[j] = *(const u32x2*)(x + (size_t)token * D_ + nt * 80 + j * 16 + 4 * g);
    br[j] = *(const u32x2*)(b2 + nt * 80 + j * 16 + 4 * g);
  }

  __syncthreads();
#pragma unroll
  for (int i = 0; i < 4; ++i) {
    if (i == wid) continue;
    const int src = (wid - i + 3) & 3;
#pragma unroll
    for (int j = 0; j < 5; ++j) *(LAS f32x4*)(smem + (((i * 3 + (src > 2 ? 2 : src)) * 5 + j) * 64 + lane) * 16) = acc[i][j];
  }
  __syncthreads();
  f32x4 fin[5];
#pragma unroll
  for (int j = 0; j < 5; ++j) fin[j] = wid == 0 ? acc[0][j] : wid == 1 ? acc[1][j] : wid == 2 ? acc[2][j] : acc[3][j];
#pragma unroll
  for (int sidx = 0; sidx < 3; ++sidx)
#pragma unroll
    for (int j = 0; j < 5; ++j) fin[j] += *(const LAS f32x4*)(smem + (((wid * 3 + sidx) * 5 + j) * 64 + lane) * 16);
#pragma unroll
  for (int j = 0; j < 5; ++j) {
    const float o0 = fin[j][0] + bflo(xr[j][0]) + bflo(br[j][0]);
    const float o1 = fin[j][1] + bfhi(xr[j][0]) + bfhi(br[j][0]);
    const float o2 = fin[j][2] + bflo(xr[j][1]) + bflo(br[j][1]);
    const float o3 = fin[j][3] + bfhi(xr[j][1]) + bfhi(br[j][1]);
    *(u32x2*)(out + (size_t)token * D_ + nt * 80 + j * 16 + 4 * g) = (u32x2){pack2(o0, o1), pack2(o2, o3)};
  }
}

}

extern "C" void kernel_launch(void* const* d_in, const int* in_sizes, int n_in, void* d_out, int out_size, void* d_ws,
                              size_t ws_size, hipStream_t stream) {
  const __hip_bfloat16* x = (const __hip_bfloat16*)d_in[0];
  const __hip_bfloat16* wqkv = (const __hip_bfloat16*)d_in[1];
  const __hip_bfloat16* wo = (const __hip_bfloat16*)d_in[2];
  const __hip_bfloat16* w1 = (const __hip_bfloat16*)d_in[4];
  const __hip_bfloat16* b1 = (const __hip_bfloat16*)d_in[5];
  const __hip_bfloat16* w2 = (const __hip_bfloat16*)d_in[6];
  const __hip_bfloat16* b2 = (const __hip_bfloat16*)d_in[7];
  (void)hipFuncSetAttribute((const void*)k1_kernel, hipFuncAttributeMaxDynamicSharedMemorySize, K1_LDS);
  (void)hipFuncSetAttribute((const void*)k2_kernel, hipFuncAttributeMaxDynamicSharedMemorySize, K2_LDS);
  k1_kernel<<<256, 512, K1_LDS, stream>>>(x, wqkv, w1, b1, w2, (char*)d_ws);
  k2_kernel<<<256, 256, K2_LDS, stream>>>(x, wo, b2, (char*)d_ws, (__hip_bfloat16*)d_out);
}
```

```cpp
#include <hip/hip_runtime.h>
#include <hip/hip_bf16.h>
#include <stdint.h>

typedef __attribute__((ext_vector_type(8))) __bf16 bf16x8;
typedef __attribute__((ext_vector_type(4))) __bf16 bf16x4;
typedef __attribute__((ext_vector_type(2))) __bf16 bf16x2;
typedef __attribute__((ext_vector_type(4))) float f32x4;
typedef __attribute__((ext_vector_type(2))) float f32x2;
typedef __attribute__((ext_vector_type(4))) unsigned u32x4;
typedef __attribute__((ext_vector_type(2))) unsigned u32x2;
#define LAS __attribute__((address_space(3)))
#define GAS __attribute__((address_space(1)))
typedef GAS unsigned gu32;
typedef GAS unsigned long long gu64;
#define SB() __builtin_amdgcn_sched_barrier(0)

#define STAMP(k)
namespace {

constexpr int S_ = 2048, D_ = 640, F_ = 1536;
constexpr int NU = 68;
constexpr size_t OFF_AF = 0;
constexpr size_t OFF_BF = OFF_AF + (size_t)128 * NU * 1024;
constexpr size_t OFF_KVP = OFF_BF + (size_t)40 * NU * 1024;
constexpr size_t OFF_ZF = OFF_KVP + (size_t)32 * 16 * 1680;
constexpr size_t OFF_FLAG = OFF_ZF + (size_t)8 * 640 * 2;
constexpr size_t WS_BYTES = OFF_FLAG + 8 * 32 * 4;

constexpr int K1_XB = 16384;
constexpr int K1_STAGE = K1_XB + 30720;
constexpr int K1_PF = 3 * K1_STAGE;
constexpr int K1_ST = K1_PF + 2048;
constexpr int K1_ONES = K1_ST + 2560;
constexpr int K1_LDS = K1_ONES + 16;

__device__ __forceinline__ unsigned afrag(int m16, int u) { return (unsigned)((((m16 >> 2) * NU + u) * 4 + (m16 & 3)) * 1024); }
__device__ __forceinline__ unsigned bfrag(int n16, int u) {
  const int n80 = n16 / 5;
  return (unsigned)(((n80 * NU + u) * 5 + (n16 - n80 * 5)) * 1024);
}

template <int V>
struct IntC {
  static constexpr int value = V;
};
template <int B, int E, class F>
__device__ __forceinline__ void static_for(F&& f) {
  if constexpr (B < E) {
    f(IntC<B>{});
    static_for<B + 1, E>(f);
  }
}

__device__ __forceinline__ u32x4 make_rsrc(const void* p, unsigned bytes) {
  const unsigned long long a = (unsigned long long)p;
  u32x4 r;
  r[0] = (unsigned)a;
  r[1] = (unsigned)(a >> 32) & 0xffffu;
  r[2] = bytes;
  r[3] = 0x00020000u;
  return r;
}

__device__ __forceinline__ void dma16(u32x4 rsrc, unsigned voff, unsigned soff, unsigned lds) {
  unsigned keep;
  asm volatile(
      "s_mov_b32 %0, m0\n\t"
      "s_mov_b32 m0, %4\n\t"
      "s_nop 0\n\t"
      "buffer_load_dwordx4 %1, %2, %3 offen lds\n\t"
      "s_mov_b32 m0, %0"
      : "=&s"(keep)
      : "v"(voff), "s"(rsrc), "s"(soff), "s"(lds)
      : "memory");
}

__device__ __forceinline__ void dma4(u32x4 rsrc, unsigned voff, unsigned soff, unsigned lds) {
  unsigned keep;
  asm volatile(
      "s_mov_b32 %0, m0\n\t"
      "s_mov_b32 m0, %4\n\t"
      "s_nop 0\n\t"
      "buffer_load_dword %1, %2, %3 offen lds\n\t"
      "s_mov_b32 m0, %0"
      : "=&s"(keep)
      : "v"(voff), "s"(rsrc), "s"(soff), "s"(lds)
      : "memory");
}

__device__ __forceinline__ unsigned pack2(float a, float b) {
  f32x2 p = {a, b};
  return __builtin_bit_cast(unsigned, __builtin_convertvector(p, bf16x2));
}
__device__ __forceinline__ float bflo(unsigned u) { return __builtin_bit_cast(float, u << 16); }
__device__ __forceinline__ float bfhi(unsigned u) { return __builtin_bit_cast(float, u & 0xffff0000u); }
__device__ __forceinline__ float dot2(unsigned a, unsigned b, float c) {
  return __builtin_amdgcn_fdot2_f32_bf16(__builtin_bit_cast(bf16x2, a), __builtin_bit_cast(bf16x2, b), c, false);
}
__device__ __forceinline__ f32x4 mfma16(bf16x8 a, bf16x8 b, f32x4 c) {
  return __builtin_amdgcn_mfma_f32_16x16x32_bf16(a, b, c, 0, 0, 0);
}
__device__ __forceinline__ bf16x8 tr_frag(const LAS char* p0, const LAS char* p1) {
  bf16x4 lo = __builtin_amdgcn_ds_read_tr16_b64_v4bf16((LAS bf16x4*)p0);
  bf16x4 hi = __builtin_amdgcn_ds_read_tr16_b64_v4bf16((LAS bf16x4*)p1);
  return __builtin_shufflevector(lo, hi, 0, 1, 2, 3, 4, 5, 6, 7);
}
__device__ __forceinline__ float phi(float t) {
  const float e = __builtin_amdgcn_exp2f(t * 1.4426950408889634f);
  return (t > 0.f ? t + 1.f : e) + 1e-6f;
}
__device__ __forceinline__ float gelu_tanh(float x) {
  const float c1 = -2.f * 0.7978845608028654f * 1.4426950408889634f;
  const float c2 = c1 * 0.044715f;
  const float t = x * __builtin_fmaf(x * x, c2, c1);
  const float e = __builtin_amdgcn_exp2f(t);
  return x * __builtin_amdgcn_rcpf(1.f + e);
}

template <bool QKV>
__device__ __forceinline__ void k1_body(const __hip_bfloat16* __restrict__ x, const __hip_bfloat16* __restrict__ w,
                                        const __hip_bfloat16* __restrict__ b1, const __hip_bfloat16* __restrict__ w2,
                                        char* __restrict__ ws, int mtile, int grp, int sel, LAS char* smem) {
  constexpr int CPR = QKV ? 30 : 24;
  constexpr int RS = CPR * 16;
  constexpr int LDW = QKV ? 1920 : 1536;
  constexpr int NWS = QKV ? 4 : 3;
  constexpr int NTW = QKV ? 4 : 3;
  constexpr int NDMA = NWS + 2;
  constexpr int PKID = 0;
  STAMP(0);

  const int tid = threadIdx.x, lane = tid & 63;
  const int wid = __builtin_amdgcn_readfirstlane(tid >> 6);
  const int mw = wid >> 2, nw = wid & 3;
  const int l15 = lane & 15, g = lane >> 4;
  const unsigned lds0 = (unsigned)(size_t)smem;

  const u32x4 rx = make_rsrc(x, S_ * D_ * 2);
  const u32x4 rw = make_rsrc(w, D_ * LDW * 2);

  unsigned pfoff;
  {
    const int mi = mtile & 3;
    if (mw == 0) {
      pfoff = (unsigned)((mtile * 128 + 16 * sel + 4 * nw + (lane & 3)) * D_ * 2);
    } else {
      const int row = 16 * mi + 4 * nw + (lane >> 4);
      int j = lane & 15;
      if (QKV) {
        j = j > 8 ? 8 : j;
        const int part = j / 3, wi = j - part * 3;
        pfoff = (unsigned)(row * LDW * 2 + part * 1280 + grp * 160 + (wi == 2 ? 156 : wi * 128));
      } else {
        j = j > 2 ? 2 : j;
        pfoff = (unsigned)(row * LDW * 2 + grp * 384 + j * 128);
      }
    }
  }
  const u32x4 rpf = mw == 0 ? rx : rw;
  const unsigned pfstep = mw == 0 ? 128u : (unsigned)(64 * LDW * 2);
  const unsigned pflds = lds0 + K1_PF + wid * 256;
#define K1PFMIN 0
#define K1PFMAX 10
  auto prefetch = [&](int s) { dma4(rpf, pfoff, s < K1PFMAX && s >= K1PFMIN ? (unsigned)s * pfstep : 0x40000000u, pflds); };
  prefetch(0);
  prefetch(1);

  unsigned xoff[2], woff[NWS];
#pragma unroll
  for (int j = 0; j < 2; ++j) {
    const int piece = 2 * wid + j, row = piece * 8 + (lane >> 3), ch = (lane & 7) ^ (row & 7);
    xoff[j] = (unsigned)(((mtile * 128 + row) * D_ + ch * 8) * 2);
  }
#pragma unroll
  for (int j = 0; j < NWS; ++j) {
    int piece = wid + 8 * j;
    if (QKV && piece > 29) piece = 29;
    const int ci = piece * 64 + lane, rho = ci / CPR, cp = ci - rho * CPR;
    const int kl = (rho & ~12) | ((rho & 4) << 1) | ((rho & 8) >> 1);
    int col;
    if (QKV) {
      const int part = cp / 10, wi = cp - part * 10;
      col = part * 640 + grp * 80 + wi * 8;
    } else {
      const int c = cp ^ (2 * ((rho >> 1) & 3));
      col = grp * 192 + c * 8;
    }
    woff[j] = (unsigned)((kl * LDW + col) * 2);
  }
  auto memop = [&](int op, int sd, unsigned bufoff) {
    if (op < 2) {
      dma16(rx, xoff[op], sd < 10 ? (unsigned)(sd * 128) : 0x40000000u, lds0 + bufoff + (2 * wid + op) * 1024);
    } else if (op < NDMA) {
      int piece = wid + 8 * (op - 2);
      if (QKV && piece > 29) piece = 29;
      dma16(rw, woff[op - 2], (unsigned)(sd * 64 * LDW * 2), lds0 + bufoff + K1_XB + piece * 1024);
    } else if (op == NDMA) {
      prefetch(sd + 2);
    }
  };
  unsigned w2r[8];
  int job = (mtile * 8 + grp) * 8 + wid;
  u32x2 b1r[3];
  if (!QKV) {
#pragma unroll
    for (int t = 0; t < 3; ++t) b1r[t] = *(const u32x2*)(b1 + (grp * 12 + nw * 3 + (t + 2 * mw) % 3) * 16 + 4 * g);
    const int jc = job < 960 ? job : 959;
    const int u = jc / 20, nb = jc - u * 20;
#pragma unroll
    for (int j = 0; j < 8; ++j) {
      const int k = 32 * u + 8 * g + j;
      w2r[j] = *(const unsigned*)(w2 + (size_t)k * D_ + nb * 32 + 2 * l15);
    }
  }

  asm volatile("s_waitcnt vmcnt(%0)" ::"n"(QKV ? 1 : 12) : "memory");
  STAMP(2);
#pragma unroll
  for (int op = 0; op <= NDMA; ++op) memop(op, 0, 0);
  STAMP(3);
#pragma unroll
  for (int op = 0; op <= NDMA; ++op) memop(op, 1, K1_STAGE);
  STAMP(4);

  int xrd0[4], xrd1[4];
#pragma unroll
  for (int i = 0; i < 4; ++i) {
    const int rb = (mw * 64 + ((i + nw) & 3) * 16 + l15) * 128;
    xrd0[i] = rb + ((g ^ (lane & 7)) << 4);
    xrd1[i] = rb + (((4 + g) ^ (lane & 7)) << 4);
  }
  const int ga = g >> 1, gb = g & 1, dq = (lane >> 2) & 3, p = lane & 3;
  const int rho0 = 16 * ga + 4 * gb + dq;
  int wrd[NTW], tsl[NTW];
  unsigned wmask[NTW];
#pragma unroll
  for (int t = 0; t < NTW; ++t) {
    int T;
    if (QKV) {
      const int kind = t ^ (2 * mw);
      T = kind < 3 ? 5 * kind + nw : 5 * nw + 4;
    } else {
      T = nw * 3 + (t + 2 * mw) % 3;
    }
    tsl[t] = T;
    int ch = 2 * T + (p >> 1);
    if (!QKV) ch ^= 2 * ((rho0 >> 1) & 3);
    wrd[t] = K1_XB + rho0 * RS + ch * 16 + (p & 1) * 8;
    wmask[t] = ~0u;
    if (QKV && (t & 1) && T == 19) {
      wrd[t] = K1_ONES;
      wmask[t] = 0u;
    }
  }
  if (QKV && tid < 4) *(LAS unsigned*)(smem + K1_ONES + tid * 4) = 0x3f803f80u;

  f32x4 acc[4][NTW];
#pragma unroll
  for (int i = 0; i < 4; ++i)
#pragma unroll
    for (int t = 0; t < NTW; ++t) acc[i][t] = (f32x4){0.f, 0.f, 0.f, 0.f};
  f32x4 gram = {0.f, 0.f, 0.f, 0.f}, rsum = {0.f, 0.f, 0.f, 0.f};
  f32x4 cm0 = {0.f, 0.f, 0.f, 0.f}, cm1 = {0.f, 0.f, 0.f, 0.f};
  const unsigned ones = 0x3f803f80u;
  u32x4 onesu = {ones, ones, ones, ones};
  asm volatile("" : "+v"(onesu));
  const bf16x8 onesv = __builtin_bit_cast(bf16x8, onesu);

  bf16x8 xf0[4], wf0[NTW], xf1[4], wf1[NTW];
  auto rdw = [&](bf16x8(&wf)[NTW], unsigned sbo, int kk, int t) {
    if (t >= NTW) return;
    if (QKV && (t & 1)) {
      const unsigned o0 = (sbo + kk * 32 * RS) & wmask[t], o1 = (sbo + kk * 32 * RS + 8 * RS) & wmask[t];
      wf[t] = tr_frag(smem + wrd[t] + o0, smem + wrd[t] + o1);
    } else {
      wf[t] = tr_frag(smem + sbo + wrd[t] + kk * 32 * RS, smem + sbo + wrd[t] + kk * 32 * RS + 8 * RS);
    }
  };
  auto rdx = [&](bf16x8(&xf)[4], unsigned sbo, int kk, int i) {
    xf[i] = *(const LAS bf16x8*)(smem + sbo + (kk ? xrd1[i] : xrd0[i]));
  };
  auto compute = [&](bf16x8(&xf)[4], bf16x8(&wf)[NTW], bf16x8(&xn)[4], bf16x8(&wn)[NTW], unsigned sb, int kk,
                     int op0, int sd, unsigned bufoff) {
#pragma unroll
    for (int t = 0; t < 3; ++t) {
      acc[0][t] = mfma16(wf[t], xf[0], acc[0][t]);
      acc[1][t] = mfma16(wf[t], xf[1], acc[1][t]);
      SB();
      rdw(wn, sb, kk, t);
      if (t < 2) {
        rdx(xn, sb, kk, 2 * t);
        rdx(xn, sb, kk, 2 * t + 1);
      } else {
        rdw(wn, sb, kk, 3);
      }
      SB();
      acc[2][t] = mfma16(wf[t], xf[2], acc[2][t]);
      acc[3][t] = mfma16(wf[t], xf[3], acc[3][t]);
      if (t == 0) gram = mfma16(xf[0], xf[0], gram);
      SB();
      memop(op0 + t, sd, bufoff);
      SB();
    }
    if (QKV) {
#pragma unroll
      for (int i = 0; i < 4; ++i) acc[i][NTW - 1] = mfma16(wf[NTW - 1], xf[i], acc[i][NTW - 1]);
    } else {
      rsum = mfma16(onesv, xf[0], rsum);
    }
    cm0 = mfma16(wf[0], onesv, cm0);
    cm1 = mfma16(wf[1], onesv, cm1);
    SB();
    memop(op0 + 3, sd, bufoff);
    SB();
  };

  STAMP(5);
  asm volatile("s_waitcnt vmcnt(%0) lgkmcnt(0)\n\ts_barrier" ::"n"(NDMA + 2) : "memory");
  STAMP(1);
#pragma unroll
  for (int t = 0; t < 4; ++t) {
    rdw(wf0, 0u, 0, t);
    rdx(xf0, 0u, 0, t);
  }
#pragma unroll
  for (int op = 0; op < 4; ++op) memop(op, 2, 2 * K1_STAGE);
  unsigned bufc = 0, bufo = 2 * K1_STAGE;
#pragma unroll 1
  for (int s = 0; s < 10; ++s) {
    compute(xf0, wf0, xf1, wf1, bufc, 1, 4, s + 2, bufo);
    bufo = bufc;
    bufc = bufc == 2 * K1_STAGE ? 0 : bufc + K1_STAGE;
    __builtin_amdgcn_s_waitcnt(0xC07F);
    asm volatile("s_waitcnt vmcnt(%0)\n\ts_barrier" ::"n"(NDMA + 2) : "memory");
    compute(xf1, wf1, xf0, wf0, bufc, 0, 0, s + 3, bufo);
  }
  STAMP(11);

  LAS float* st_sum = (LAS float*)(smem + K1_ST);
  LAS float* st_sq = st_sum + 128;
  LAS float* st_cs = st_sum + 256;
  {
    const int r = l15 & 3;
    const float dg = r == 0 ? gram[0] : r == 1 ? gram[1] : r == 2 ? gram[2] : gram[3];
    if (g == (l15 >> 2)) st_sq[(4 * mw + nw) * 16 + l15] = dg;
    if (l15 == 0) {
      *(LAS f32x4*)(st_cs + tsl[0] * 16 + 4 * g) = cm0;
      *(LAS f32x4*)(st_cs + tsl[1] * 16 + 4 * g) = cm1;
    }
    if (QKV) {
      if (mw == 1) {
        asm volatile("" ::: "memory");
#pragma unroll
        for (int i = 0; i < 4; ++i) {
          const f32x4 t0 = acc[i][0], t1 = acc[i][1];
          acc[i][0] = acc[i][2];
          acc[i][1] = acc[i][NTW - 1];
          acc[i][2] = t0;
          acc[i][NTW - 1] = t1;
        }
      }
      if (g == 0 && nw == 3) {
#pragma unroll
        for (int i = 0; i < 4; ++i) st_sum[(4 * mw + ((i + nw) & 3)) * 16 + l15] = acc[i][NTW - 1][0];
      }
    } else {
      if (g == 0) st_sum[(4 * mw + nw) * 16 + l15] = rsum[0];
    }
  }
  asm volatile("s_waitcnt vmcnt(0) lgkmcnt(0)\n\ts_barrier" ::: "memory");
  float mr[4], rstd[4];
#pragma unroll
  for (int i = 0; i < 4; ++i) {
    const int row = (4 * mw + ((i + nw) & 3)) * 16 + l15;
    const float mean = st_sum[row] * (1.f / 640.f);
    float var = __builtin_fmaf(st_sq[row], 1.f / 640.f, -mean * mean);
    var = var > 0.f ? var : 0.f;
    float r = __builtin_amdgcn_rsqf(var + 1e-5f);
    if (!QKV) r *= __builtin_amdgcn_rsqf(var * r * r + 1e-5f);
    rstd[i] = r;
    mr[i] = -mean * r;
  }
  const __amdgpu_buffer_rsrc_t rws = __builtin_amdgcn_make_buffer_rsrc(ws, 0, (unsigned)WS_BYTES, 0x00020000);
#define STAUX 16
  auto store_pair = [&](int i, int ubase, int Tg, u32x2 a, u32x2 b) {
    const auto s0 = __builtin_amdgcn_permlane16_swap(a[0], b[0], false, false);
    const auto s1 = __builtin_amdgcn_permlane16_swap(a[1], b[1], false, false);
    const u32x4 o = {s0[0], s1[0], s0[1], s1[1]};
    const int m16 = mtile * 8 + mw * 4 + ((i + (g & 1) + nw) & 3);
    const unsigned voff = afrag(m16, 0) + ((g >> 1) * 16 + l15) * 16 + (unsigned)OFF_AF + (ubase + (Tg >> 1)) * 4096 + (Tg & 1) * 512;
    __builtin_amdgcn_raw_buffer_store_b128(o, rws, voff, 0, STAUX);
  };
  STAMP(12);

  if (!QKV) {
#pragma unroll
    for (int t = 0; t < NTW; ++t) {
      const int Tg = grp * 12 + tsl[t];
      const f32x4 cs = *(const LAS f32x4*)(st_cs + tsl[t] * 16 + 4 * g);
      const float bv[4] = {bflo(b1r[t][0]), bfhi(b1r[t][0]), bflo(b1r[t][1]), bfhi(b1r[t][1])};
      u32x2 o[4];
#pragma unroll
      for (int i = 0; i < 4; ++i) {
        float hv[4];
#pragma unroll
        for (int r = 0; r < 4; ++r)
          hv[r] = gelu_tanh(__builtin_fmaf(acc[i][t][r], rstd[i], __builtin_fmaf(mr[i], cs[r], bv[r])));
        o[i] = (u32x2){pack2(hv[0], hv[1]), pack2(hv[2], hv[3])};
        if (i & 1) store_pair(i - 1, 0, Tg, o[i - 1], o[i]);
      }
    }
    if (job < 960) {
      const int u = job / 20, nb = job - u * 20;
      u32x4 lo, hi;
#pragma unroll
      for (int j = 0; j < 4; ++j) {
        lo[j] = __builtin_amdgcn_perm(w2r[2 * j + 1], w2r[2 * j], 0x05040100u);
        hi[j] = __builtin_amdgcn_perm(w2r[2 * j + 1], w2r[2 * j], 0x07060302u);
      }
      const int n16 = nb * 2 + (l15 >> 3), nl = (2 * l15) & 15;
      const unsigned dst = (unsigned)OFF_BF + bfrag(n16, u) + (g * 16 + nl) * 16;
      __builtin_amdgcn_raw_buffer_store_b128(lo, rws, dst, 0, 0);
      __builtin_amdgcn_raw_buffer_store_b128(hi, rws, dst + 16, 0, 0);
    }
  } else {
    constexpr int PVS = 352;
    auto tile = [&](int t, int T, bool isphi, bool isq) {
      const f32x4 cs = *(const LAS f32x4*)(st_cs + T * 16 + 4 * g);
      u32x2 o[4];
#pragma unroll
      for (int i = 0; i < 4; ++i) {
        float v[4];
#pragma unroll
        for (int r = 0; r < 4; ++r) {
          if (isphi) {
            const float tp = __builtin_fmaf(acc[i][t][r], rstd[i], __builtin_fmaf(mr[i], cs[r], 1e-6f));
            const float e = __builtin_amdgcn_exp2f(__builtin_fmaf(tp, 1.4426950408889634f, -1.4426950408889634e-6f));
            v[r] = __builtin_amdgcn_fmed3f(e, 0.f, 1.f) + __builtin_fmaxf(tp, 1e-6f);
          } else {
            v[r] = __builtin_fmaf(acc[i][t][r], rstd[i], mr[i] * cs[r]);
          }
        }
        o[i] = (u32x2){pack2(v[0], v[1]), pack2(v[2], v[3])};
        if (isq) {
          if (i & 1) store_pair(i - 1, 48, grp * 5 + T, o[i - 1], o[i]);
        } else {
          *(LAS u32x2*)(smem + (mw * 64 + ((i + nw) & 3) * 16 + l15) * PVS + (T - 5) * 32 + g * 8) = o[i];
        }
      }
    };
    tile(1, 5 + nw, true, false);
    tile(2, 10 + nw, false, false);
    if (nw == 1) tile(3, 9, true, false);
    if (nw == 2) tile(3, 14, false, false);
    asm volatile("s_waitcnt lgkmcnt(0)\n\ts_barrier" ::: "memory");
    if (nw >= 2 || wid == 1) {
      const int dt = nw >= 2 ? 2 * mw + nw - 2 : 4;
      const int trow = (4 * g + dq) * PVS + (p >> 1) * 16 + (p & 1) * 8;
      f32x4 kvacc[3], zacc = {0.f, 0.f, 0.f, 0.f};
#pragma unroll
      for (int j = 0; j < 3; ++j) kvacc[j] = (f32x4){0.f, 0.f, 0.f, 0.f};
#pragma unroll
      for (int ks = 0; ks < 4; ++ks) {
        const LAS char* pb = smem + ks * 32 * PVS + trow;
        const bf16x8 a = tr_frag(pb + dt * 32, pb + dt * 32 + 16 * PVS);
        zacc = mfma16(a, onesv, zacc);
#pragma unroll
        for (int j = 0; j < 3; ++j) {
          int et = dt - 1 + j;
          et = et < 0 ? 0 : (et > 4 ? 4 : et);
          const bf16x8 b = tr_frag(pb + 160 + et * 32, pb + 160 + et * 32 + 16 * PVS);
          kvacc[j] = mfma16(a, b, kvacc[j]);
        }
      }
      const int dl = dt * 16 + 4 * g, hd = dl / 20, dg = (dl - hd * 20) >> 2;
      const unsigned kvp = (unsigned)OFF_KVP + (unsigned)(((grp * 4 + hd) * 16 + mtile) * 1680);
#pragma unroll
      for (int j = 0; j < 3; ++j) {
        const int e = (dt - 1 + j) * 16 + l15 - hd * 20;
        if (e >= 0 && e < 20)
          __builtin_amdgcn_raw_buffer_store_b128(__builtin_bit_cast(u32x4, kvacc[j]), rws, kvp + (dg * 20 + e) * 16, 0, STAUX);
      }
      if (l15 == 0) __builtin_amdgcn_raw_buffer_store_b128(__builtin_bit_cast(u32x4, zacc), rws, kvp + (100 + dg) * 16, 0, STAUX);
    }
    tile(0, nw, true, true);
    if (nw == 0) tile(3, 4, true, true);
  }
  STAMP(13);
  STAMP(14);
}

__global__ void __launch_bounds__(512, 2)
k1_kernel(const __hip_bfloat16* __restrict__ x, const __hip_bfloat16* __restrict__ wqkv,
          const __hip_bfloat16* __restrict__ w1, const __hip_bfloat16* __restrict__ b1,
          const __hip_bfloat16* __restrict__ w2, char* __restrict__ ws) {
  extern __shared__ __attribute__((aligned(16))) char smem_[];
  LAS char* smem = (LAS char*)smem_;
  const int b = blockIdx.x, xcd = b & 7, idx = b >> 3;
  const int mtile = (xcd & 3) * 4 + (idx & 3), sel = idx >> 2;
  const int grp = (xcd >> 2) * 4 + (sel & 3);
  if (threadIdx.x < 200) {
    const int c = b * 200 + threadIdx.x, n80 = c / 6400, r = c - n80 * 6400;
    *(u32x4*)(ws + OFF_BF + (size_t)(n80 * NU + 48) * 5120 + r * 16) = (u32x4){~0u, ~0u, ~0u, ~0u};
  }
  if (sel >> 2)
    k1_body<false>(x, w1, b1, w2, ws, mtile, grp, sel, smem);
  else
    k1_body<true>(x, wqkv, b1, w2, ws, mtile, grp, sel, smem);
}

constexpr int K2_RED = 81920;
constexpr int K2_KVT = K2_RED + 8192;
constexpr int K2_WOS = K2_KVT + 1600;
constexpr int K2_ZB = K2_WOS + 3200;
constexpr int K2_CNT = K2_ZB + 1280;
constexpr int K2_LDS = K2_CNT + 16;

__device__ __forceinline__ unsigned helper_arrive(LAS unsigned* c, int lane) {
  unsigned old = 0;
  __builtin_amdgcn_s_waitcnt(0xC07F);
  if (lane == 0) old = __hip_atomic_fetch_add(c, 1u, __ATOMIC_RELAXED, __HIP_MEMORY_SCOPE_WORKGROUP);
  return __builtin_amdgcn_readfirstlane(old);
}
__device__ __forceinline__ void helper_wait(LAS unsigned* c) {
  while (__builtin_amdgcn_readfirstlane(__hip_atomic_load(c, __ATOMIC_RELAXED, __HIP_MEMORY_SCOPE_WORKGROUP)) < 4u)
    __builtin_amdgcn_s_sleep(1);
  asm volatile("" ::: "memory");
}

__global__ void __launch_bounds__(512, 2)
k2_kernel(const __hip_bfloat16* __restrict__ x, const __hip_bfloat16* __restrict__ wo,
          const __hip_bfloat16* __restrict__ b2, char* __restrict__ ws, __hip_bfloat16* __restrict__ out) {
  extern __shared__ __attribute__((aligned(16))) char smem_[];
  LAS char* smem = (LAS char*)smem_;
  const int tid = threadIdx.x, lane = tid & 63;
  const int wid = __builtin_amdgcn_readfirstlane(tid >> 6);
  const int w = wid & 3, helper = wid >> 2;
  const int l15 = lane & 15, g = lane >> 4;
  const int b = blockIdx.x, xcd = b & 7, idx = b >> 3;
  const int mt = (xcd & 3) * 8 + (idx & 7);
  const int nt = (xcd >> 2) * 4 + (idx >> 3);
  const int ph = xcd * 4 + (idx & 3), ps = idx >> 2;
  constexpr int PKID = 1;
  STAMP(0);

  const __amdgpu_buffer_rsrc_t rab = __builtin_amdgcn_make_buffer_rsrc(ws + OFF_AF, 0, 168 * NU * 1024, 0x00020000);
  const unsigned aoff = (unsigned)(mt * 4 * NU * 1024 + lane * 16);
  const unsigned boff = (unsigned)((128 + nt * 5) * NU * 1024 + lane * 16);
  const int u0 = w * 12, uo = 48 + w * 5;
  LAS unsigned* cnt = (LAS unsigned*)(smem + K2_CNT);
  const int token = mt * 64 + w * 16 + l15;
  u32x2 xr[3], br[3];
  auto load_resid = [&]() {
#pragma unroll
    for (int j = 0; j < 3; ++j) {
      const int jj = helper * 3 + j < 5 ? helper * 3 + j : 4;
      xr[j] = *(const u32x2*)(x + (size_t)token * D_ + nt * 80 + jj * 16 + 4 * g);
      br[j] = *(const u32x2*)(b2 + nt * 80 + jj * 16 + 4 * g);
    }
  };

  if (helper == 0) {
    u32x4 fa[4][4], fb[4][5];
    auto load_one = [&](int s, int n) {
      const int slot = s & 3;
      if (s < 12) {
        if (n < 5)
          fb[slot][n] = __builtin_amdgcn_raw_buffer_load_b128(rab, boff + n * 1024, (u0 + s) * 5120, 0);
        else
          fa[slot][n - 5] = __builtin_amdgcn_raw_buffer_load_b128(rab, aoff + (n - 5) * 1024, (u0 + s) * 4096, 0);
      } else if (s < 17) {
        if (n < 5)
          fb[slot][n] = __builtin_amdgcn_raw_buffer_load_b128(rab, boff + n * 1024, (uo + s - 12) * 5120, 16);
      }
    };
    asm volatile("s_barrier" ::: "memory");
#pragma unroll
    for (int k = 0; k < 3; ++k)
#pragma unroll
      for (int n = 0; n < 9; ++n) load_one(k, n);
    SB();
    STAMP(11);
    f32x4 acc[4][5];
#pragma unroll
    for (int i = 0; i < 4; ++i)
#pragma unroll
      for (int j = 0; j < 5; ++j) acc[i][j] = (f32x4){0.f, 0.f, 0.f, 0.f};
    auto step = [&](auto sc_) {
      constexpr int s = decltype(sc_)::value;
      constexpr int slot = s & 3;
      if constexpr (s == 9) STAMP(2);
      if constexpr (s == 11) {
        STAMP(4);
        asm volatile("s_barrier" ::: "memory");
        STAMP(5);
      }
      if constexpr (s >= 12) {
        auto missing = [&]() {
          unsigned m = 0;
#pragma unroll
          for (int j = 0; j < 5; ++j) m |= (fb[slot][j][0] == ~0u) | (fb[slot][j][2] == ~0u);
          return __any(m);
        };
        if (__builtin_expect(missing(), 0)) {
          unsigned spins = 0;
          do {
            __builtin_amdgcn_s_sleep(2);
#pragma unroll
            for (int j = 0; j < 5; ++j) load_one(s, j);
          } while (missing() && ++spins < (1u << 20));
        }
      }
      if constexpr (s >= 11 && s < 16) {
#pragma unroll
        for (int i = 0; i < 4; ++i)
          fa[(s + 1) & 3][i] = *(const LAS u32x4*)(smem + ((w * 5 + s - 11) * 4 + i) * 1024 + lane * 16);
      }
      SB();
#pragma unroll
      for (int q = 0; q < 10; ++q) {
#pragma unroll
        for (int h2 = 0; h2 < 2; ++h2) {
          const int i = (2 * q + h2) / 5, j = (2 * q + h2) % 5;
          acc[i][j] = mfma16(__builtin_bit_cast(bf16x8, fb[slot][j]), __builtin_bit_cast(bf16x8, fa[slot][i]), acc[i][j]);
        }
        SB();
        if (q < 9) load_one(s + 3, q);
        SB();
      }
      if constexpr (s == 0) STAMP(13);
      if constexpr (s == 13) load_resid();
    };
    static_for<0, 17>(step);
    SB();
    STAMP(6);
    asm volatile("s_barrier" ::: "memory");
#pragma unroll
    for (int i = 0; i < 4; ++i)
#pragma unroll
      for (int j = 0; j < 5; ++j) *(LAS f32x4*)(smem + (((i * 5 + j) * 4 + w) * 64 + lane) * 16) = acc[i][j];
  } else {
    const int ht = tid - 256;
    __builtin_amdgcn_s_setprio(3);
    LAS float* kvt = (LAS float*)(smem + K2_KVT);
    LAS char* wos = smem + K2_WOS;
    LAS char* red = smem + K2_RED;
    f32x4 kvl[4][2];
    {
      const char* src = ws + OFF_KVP + (size_t)(ph * 16 + w * 4) * 1680;
#pragma unroll
      for (int pidx = 0; pidx < 4; ++pidx) {
        kvl[pidx][0] = *(const f32x4*)(src + pidx * 1680 + lane * 16);
        kvl[pidx][1] = *(const f32x4*)(src + pidx * 1680 + (lane < 41 ? lane + 64 : 104) * 16);
      }
    }
    const int tc = ht < 200 ? ht : 199;
    const int pg = tc / 40, pn = tc - pg * 40;
    const u32x4 wol = *(const u32x4*)(wo + (size_t)(ph * 20 + tc / 10) * D_ + ps * 80 + (tc % 10) * 8);
    if (ht < 4) cnt[ht] = 0u;
    SB();
    asm volatile("s_waitcnt lgkmcnt(0)\n\ts_barrier" ::: "memory");
    *(LAS f32x4*)(red + (w * 128 + lane) * 16) = (kvl[0][0] + kvl[1][0]) + (kvl[2][0] + kvl[3][0]);
    *(LAS f32x4*)(red + (w * 128 + 64 + lane) * 16) = (kvl[0][1] + kvl[1][1]) + (kvl[2][1] + kvl[3][1]);
    *(LAS u32x4*)(wos + tc * 16) = wol;
    helper_arrive(cnt, lane);
    helper_wait(cnt);
    if (w < 2) {
      const f32x4 t = (*(const LAS f32x4*)(red + ht * 16) + *(const LAS f32x4*)(red + (128 + ht) * 16)) +
                      (*(const LAS f32x4*)(red + (256 + ht) * 16) + *(const LAS f32x4*)(red + (384 + ht) * 16));
      const u32x2 pk = {pack2(t[0], t[1]), pack2(t[2], t[3])};
      const int idg = ht / 20, ie = ht - idg * 20;
      if (ht < 100) *(LAS f32x4*)(kvt + ie * 20 + idg * 4) = (f32x4){bflo(pk[0]), bfhi(pk[0]), bflo(pk[1]), bfhi(pk[1])};
    }
    helper_arrive(cnt + 1, lane);
    helper_wait(cnt + 1);
    {
      f32x4 o0 = {0.f, 0.f, 0.f, 0.f}, o1 = {0.f, 0.f, 0.f, 0.f};
#pragma unroll
      for (int e = 0; e < 20; ++e) {
        const f32x4 kq = *(const LAS f32x4*)(kvt + e * 20 + pg * 4);
        const unsigned wv = *(const LAS unsigned*)(wos + e * 160 + pn * 4);
        o0 += kq * bflo(wv);
        o1 += kq * bfhi(wv);
      }
      const int q = 5 * ph + pg, n = ps * 80 + 2 * pn;
      char* dst = ws + OFF_BF + bfrag(n >> 4, 48 + (q >> 3)) + (((q >> 1) & 3) * 16 + (n & 15)) * 16 + (q & 1) * 8;
      const u32x2 v0 = {pack2(o0[0], o0[1]), pack2(o0[2], o0[3])}, v1 = {pack2(o1[0], o1[1]), pack2(o1[2], o1[3])};
      asm volatile("global_store_dwordx2 %0, %1, off sc1\n\tglobal_store_dwordx2 %0, %2, off offset:16 sc1"
                   ::"v"(dst), "v"(v0), "v"(v1) : "memory");
    }
    f32x4 zl[16];
    {
      const int lz = lane < 40 ? lane : 39;
      const char* src = ws + OFF_KVP + (size_t)((w * 8 + lz / 5) * 16) * 1680 + (100 + lz % 5) * 16;
#pragma unroll
      for (int pidx = 0; pidx < 16; ++pidx) zl[pidx] = *(const f32x4*)(src + pidx * 1680);
    }
    u32x4 pq[5][4];
#pragma unroll
    for (int ul = 0; ul < 5; ++ul)
#pragma unroll
      for (int i = 0; i < 4; ++i)
        pq[ul][i] = __builtin_amdgcn_raw_buffer_load_b128(rab, aoff + i * 1024, (uo + ul) * 4096, 0);
    SB();
    {
      f32x4 zs = zl[0];
#pragma unroll
      for (int pidx = 1; pidx < 16; ++pidx) zs += zl[pidx];
      if (lane < 40) *(LAS u32x2*)(smem + K2_ZB + w * 320 + lane * 8) = (u32x2){pack2(zs[0], zs[1]), pack2(zs[2], zs[3])};
    }
    u32x4 zp[5];
#pragma unroll
    for (int ul = 0; ul < 5; ++ul) zp[ul] = *(const LAS u32x4*)(smem + K2_ZB + w * 320 + (32 * ul + 8 * g) * 2);
    float rden[4][12];
#pragma unroll
    for (int c = 0; c < 3; ++c) {
      const int tau = 4 * c + (l15 & 3);
      const int htar = (32 * (tau >> 1) + 8 * (l15 >> 2) + 4 * (tau & 1)) / 20;
      f32x4 dacc[4];
#pragma unroll
      for (int i = 0; i < 4; ++i) dacc[i] = (f32x4){1e-6f, 1e-6f, 1e-6f, 1e-6f};
#pragma unroll
      for (int ul = 0; ul < 5; ++ul) {
        if (c == 0 && ul > 2) continue;
        if (c == 1 && ul < 1) continue;
        if (c == 2 && ul < 3) continue;
        u32x4 zf;
#pragma unroll
        for (int i = 0; i < 2; ++i) {
          const bool m = (32 * ul + 8 * g + 4 * i) / 20 == htar;
          zf[2 * i] = m ? zp[ul][2 * i] : 0u;
          zf[2 * i + 1] = m ? zp[ul][2 * i + 1] : 0u;
        }
#pragma unroll
        for (int i = 0; i < 4; ++i)
          dacc[i] = mfma16(__builtin_bit_cast(bf16x8, zf), __builtin_bit_cast(bf16x8, pq[ul][i]), dacc[i]);
      }
#pragma unroll
      for (int i = 0; i < 4; ++i)
#pragma unroll
        for (int r = 0; r < 4; ++r)
          if (4 * c + r < 10) rden[i][4 * c + r] = __builtin_amdgcn_rcpf(dacc[i][r]);
    }
#pragma unroll
    for (int ul = 0; ul < 5; ++ul)
#pragma unroll
      for (int i = 0; i < 4; ++i) {
#pragma unroll
        for (int j = 0; j < 4; ++j) {
          const float sc = rden[i][2 * ul + (j >> 1)];
          pq[ul][i][j] = pack2(bflo(pq[ul][i][j]) * sc, bfhi(pq[ul][i][j]) * sc);
        }
        *(LAS u32x4*)(smem + ((w * 5 + ul) * 4 + i) * 1024 + lane * 16) = pq[ul][i];
      }
    load_resid();
    asm volatile("s_waitcnt lgkmcnt(0)\n\ts_barrier\n\ts_barrier" ::: "memory");
  }

  asm volatile("s_waitcnt lgkmcnt(0)\n\ts_barrier" ::: "memory");
#pragma unroll
  for (int j = 0; j < 3; ++j) {
    if (j == 2 && helper) break;
    const int jj = helper * 3 + j;
    f32x4 fin = *(const LAS f32x4*)(smem + (((w * 5 + jj) * 4) * 64 + lane) * 16);
#pragma unroll
    for (int sidx = 1; sidx < 4; ++sidx) fin += *(const LAS f32x4*)(smem + (((w * 5 + jj) * 4 + sidx) * 64 + lane) * 16);
    const float o0 = fin[0] + bflo(xr[j][0]) + bflo(br[j][0]);
    const float o1 = fin[1] + bfhi(xr[j][0]) + bfhi(br[j][0]);
    const float o2 = fin[2] + bflo(xr[j][1]) + bflo(br[j][1]);
    const float o3 = fin[3] + bfhi(xr[j][1]) + bfhi(br[j][1]);
    *(u32x2*)(out + (size_t)token * D_ + nt * 80 + jj * 16 + 4 * g) = (u32x2){pack2(o0, o1), pack2(o2, o3)};
  }
  STAMP(7);
  STAMP(8);
}

}

extern "C" void kernel_launch(void* const* d_in, const int* in_sizes, int n_in, void* d_out, int out_size, void* d_ws,
                              size_t ws_size, hipStream_t stream) {
  const __hip_bfloat16* x = (const __hip_bfloat16*)d_in[0];
  const __hip_bfloat16* wqkv = (const __hip_bfloat16*)d_in[1];
  const __hip_bfloat16* wo = (const __hip_bfloat16*)d_in[2];
  const __hip_bfloat16* w1 = (const __hip_bfloat16*)d_in[4];
  const __hip_bfloat16* b1 = (const __hip_bfloat16*)d_in[5];
  const __hip_bfloat16* w2 = (const __hip_bfloat16*)d_in[6];
  const __hip_bfloat16* b2 = (const __hip_bfloat16*)d_in[7];
  (void)hipFuncSetAttribute((const void*)k1_kernel, hipFuncAttributeMaxDynamicSharedMemorySize, K1_LDS);
  (void)hipFuncSetAttribute((const void*)k2_kernel, hipFuncAttributeMaxDynamicSharedMemorySize, K2_LDS);
  k1_kernel<<<256, 512, K1_LDS, stream>>>(x, wqkv, w1, b1, w2, (char*)d_ws);
  k2_kernel<<<256, 512, K2_LDS, stream>>>(x, wo, b2, (char*)d_ws, (__hip_bfloat16*)d_out);
}
```

```cpp
#include <hip/hip_runtime.h>
#include <hip/hip_bf16.h>
#include <stdint.h>

typedef __attribute__((ext_vector_type(8))) __bf16 bf16x8;
typedef __attribute__((ext_vector_type(4))) __bf16 bf16x4;
typedef __attribute__((ext_vector_type(2))) __bf16 bf16x2;
typedef __attribute__((ext_vector_type(4))) float f32x4;
typedef __attribute__((ext_vector_type(2))) float f32x2;
typedef __attribute__((ext_vector_type(4))) unsigned u32x4;
typedef __attribute__((ext_vector_type(2))) unsigned u32x2;
#define LAS __attribute__((address_space(3)))
#define GAS __attribute__((address_space(1)))
typedef GAS unsigned gu32;
typedef GAS unsigned long long gu64;
#define SB() __builtin_amdgcn_sched_barrier(0)

#define STAMP(k)
namespace {

constexpr int S_ = 2048, D_ = 640, F_ = 1536;
constexpr int NU = 68;
constexpr size_t OFF_AF = 0;
constexpr size_t OFF_BF = OFF_AF + (size_t)128 * NU * 1024;
constexpr size_t OFF_KVP = OFF_BF + (size_t)40 * NU * 1024;
constexpr size_t OFF_ZP = OFF_KVP + (size_t)32 * 16 * 1600;
constexpr size_t WS_BYTES = OFF_ZP + (size_t)16 * 640 * 4;

constexpr int K1_XB = 16384;
constexpr int K1_STAGE = K1_XB + 30720;
constexpr int K1_PF = 3 * K1_STAGE;
constexpr int K1_ST = K1_PF + 2048;
constexpr int K1_ONES = K1_ST + 2560;
constexpr int K1_LDS = K1_ONES + 16;

__device__ __forceinline__ unsigned afrag(int m16, int u) { return (unsigned)((((m16 >> 2) * NU + u) * 4 + (m16 & 3)) * 1024); }
__device__ __forceinline__ unsigned bfrag(int n16, int u) {
  const int n80 = n16 / 5;
  return (unsigned)(((n80 * NU + u) * 5 + (n16 - n80 * 5)) * 1024);
}

template <int V>
struct IntC {
  static constexpr int value = V;
};
template <int B, int E, class F>
__device__ __forceinline__ void static_for(F&& f) {
  if constexpr (B < E) {
    f(IntC<B>{});
    static_for<B + 1, E>(f);
  }
}

__device__ __forceinline__ u32x4 make_rsrc(const void* p, unsigned bytes) {
  const unsigned long long a = (unsigned long long)p;
  u32x4 r;
  r[0] = (unsigned)a;
  r[1] = (unsigned)(a >> 32) & 0xffffu;
  r[2] = bytes;
  r[3] = 0x00020000u;
  return r;
}

__device__ __forceinline__ void dma16(u32x4 rsrc, unsigned voff, unsigned soff, unsigned lds) {
  unsigned keep;
  asm volatile(
      "s_mov_b32 %0, m0\n\t"
      "s_mov_b32 m0, %4\n\t"
      "s_nop 0\n\t"
      "buffer_load_dwordx4 %1, %2, %3 offen lds\n\t"
      "s_mov_b32 m0, %0"
      : "=&s"(keep)
      : "v"(voff), "s"(rsrc), "s"(soff), "s"(lds)
      : "memory");
}

__device__ __forceinline__ void dma4(u32x4 rsrc, unsigned voff, unsigned soff, unsigned lds) {
  unsigned keep;
  asm volatile(
      "s_mov_b32 %0, m0\n\t"
      "s_mov_b32 m0, %4\n\t"
      "s_nop 0\n\t"
      "buffer_load_dword %1, %2, %3 offen lds\n\t"
      "s_mov_b32 m0, %0"
      : "=&s"(keep)
      : "v"(voff), "s"(rsrc), "s"(soff), "s"(lds)
      : "memory");
}

__device__ __forceinline__ unsigned pack2(float a, float b) {
  f32x2 p = {a, b};
  return __builtin_bit_cast(unsigned, __builtin_convertvector(p, bf16x2));
}
__device__ __forceinline__ float bflo(unsigned u) { return __builtin_bit_cast(float, u << 16); }
__device__ __forceinline__ float bfhi(unsigned u) { return __builtin_bit_cast(float, u & 0xffff0000u); }
__device__ __forceinline__ float dot2(unsigned a, unsigned b, float c) {
  return __builtin_amdgcn_fdot2_f32_bf16(__builtin_bit_cast(bf16x2, a), __builtin_bit_cast(bf16x2, b), c, false);
}
__device__ __forceinline__ f32x4 mfma16(bf16x8 a, bf16x8 b, f32x4 c) {
  return __builtin_amdgcn_mfma_f32_16x16x32_bf16(a, b, c, 0, 0, 0);
}
__device__ __forceinline__ bf16x8 tr_frag(const LAS char* p0, const LAS char* p1) {
  bf16x4 lo = __builtin_amdgcn_ds_read_tr16_b64_v4bf16((LAS bf16x4*)p0);
  bf16x4 hi = __builtin_amdgcn_ds_read_tr16_b64_v4bf16((LAS bf16x4*)p1);
  return __builtin_shufflevector(lo, hi, 0, 1, 2, 3, 4, 5, 6, 7);
}
__device__ __forceinline__ float phi(float t) {
  const float e = __builtin_amdgcn_exp2f(t * 1.4426950408889634f);
  return (t > 0.f ? t + 1.f : e) + 1e-6f;
}
__device__ __forceinline__ float gelu_tanh(float x) {
  const float c1 = -2.f * 0.7978845608028654f * 1.4426950408889634f;
  const float c2 = c1 * 0.044715f;
  const float t = x * __builtin_fmaf(x * x, c2, c1);
  const float e = __builtin_amdgcn_exp2f(t);
  return x * __builtin_amdgcn_rcpf(1.f + e);
}

template <bool QKV>
__device__ __forceinline__ void k1_body(const __hip_bfloat16* __restrict__ x, const __hip_bfloat16* __restrict__ w,
                                        const __hip_bfloat16* __restrict__ b1, const __hip_bfloat16* __restrict__ w2,
                                        char* __restrict__ ws, int mtile, int grp, int sel, LAS char* smem) {
  constexpr int CPR = QKV ? 30 : 24;
  constexpr int RS = CPR * 16;
  constexpr int LDW = QKV ? 1920 : 1536;
  constexpr int NWS = QKV ? 4 : 3;
  constexpr int NTW = QKV ? 4 : 3;
  constexpr int NDMA = NWS + 2;
  constexpr int PKID = 0;
  STAMP(0);

  const int tid = threadIdx.x, lane = tid & 63;
  const int wid = __builtin_amdgcn_readfirstlane(tid >> 6);
  const int mw = wid >> 2, nw = mw ? 3 - (wid & 3) : wid;
  const int l15 = lane & 15, g = lane >> 4;
  const unsigned lds0 = (unsigned)(size_t)smem;

  const __amdgpu_buffer_rsrc_t rws = __builtin_amdgcn_make_buffer_rsrc(ws, 0, (unsigned)WS_BYTES, 0x00020000);
  const u32x4 rx = make_rsrc(x, S_ * D_ * 2);
  const u32x4 rw = make_rsrc(w, D_ * LDW * 2);

  unsigned pfoff;
  {
    const int mi = mtile & 3;
    if (mw == 0) {
      pfoff = (unsigned)((mtile * 128 + 16 * sel + 4 * nw + (lane & 3)) * D_ * 2);
    } else {
      const int row = 16 * mi + 4 * nw + (lane >> 4);
      int j = lane & 15;
      if (QKV) {
        j = j > 8 ? 8 : j;
        const int part = j / 3, wi = j - part * 3;
        pfoff = (unsigned)(row * LDW * 2 + part * 1280 + grp * 160 + (wi == 2 ? 156 : wi * 128));
      } else {
        j = j > 2 ? 2 : j;
        pfoff = (unsigned)(row * LDW * 2 + grp * 384 + j * 128);
      }
    }
  }
  const u32x4 rpf = mw == 0 ? rx : rw;
  const unsigned pfstep = mw == 0 ? 128u : (unsigned)(64 * LDW * 2);
  const unsigned pflds = lds0 + K1_PF + wid * 256;
#define K1PFMIN 0
#define K1PFMAX 10
  auto prefetch = [&](int s) { dma4(rpf, pfoff, s < K1PFMAX && s >= K1PFMIN ? (unsigned)s * pfstep : 0x40000000u, pflds); };
  prefetch(0);
  prefetch(1);

  unsigned xoff[2], woff[NWS];
#pragma unroll
  for (int j = 0; j < 2; ++j) {
    const int piece = 2 * wid + j, row = piece * 8 + (lane >> 3), ch = (lane & 7) ^ (row & 7);
    xoff[j] = (unsigned)(((mtile * 128 + row) * D_ + ch * 8) * 2);
  }
#pragma unroll
  for (int j = 0; j < NWS; ++j) {
    int piece = wid + 8 * j;
    if (QKV && piece > 29) piece = 29;
    const int ci = piece * 64 + lane, rho = ci / CPR, cp = ci - rho * CPR;
    const int kl = (rho & ~12) | ((rho & 4) << 1) | ((rho & 8) >> 1);
    int col;
    if (QKV) {
      const int part = cp / 10, wi = cp - part * 10;
      col = part * 640 + grp * 80 + wi * 8;
    } else {
      const int c = cp ^ (2 * ((rho >> 1) & 3));
      col = grp * 192 + c * 8;
    }
    woff[j] = (unsigned)((kl * LDW + col) * 2);
  }
  auto memop = [&](int op, int sd, unsigned bufoff) {
    if (op < 2) {
      dma16(rx, xoff[op], sd < 10 ? (unsigned)(sd * 128) : 0x40000000u, lds0 + bufoff + (2 * wid + op) * 1024);
    } else if (op < NDMA) {
      int piece = wid + 8 * (op - 2);
      if (QKV && piece > 29) piece = 29;
      dma16(rw, woff[op - 2], (unsigned)(sd * 64 * LDW * 2), lds0 + bufoff + K1_XB + piece * 1024);
    } else if (op == NDMA) {
      prefetch(sd + 2);
    }
  };
  unsigned w2r[8];
  int job = (mtile * 8 + grp) * 8 + wid;
  u32x2 b1r[3];
  if (!QKV) {
#pragma unroll
    for (int t = 0; t < 3; ++t) b1r[t] = *(const u32x2*)(b1 + (grp * 12 + nw * 3 + (t + 2 * mw) % 3) * 16 + 4 * g);
    const int jc = job < 960 ? job : 959;
    const int u = jc / 20, nb = jc - u * 20;
#pragma unroll
    for (int j = 0; j < 8; ++j) {
      const int k = 32 * u + 8 * g + j;
      w2r[j] = *(const unsigned*)(w2 + (size_t)k * D_ + nb * 32 + 2 * l15);
    }
  }

  asm volatile("s_waitcnt vmcnt(%0)" ::"n"(QKV ? 1 : 12) : "memory");
  STAMP(2);
#pragma unroll
  for (int op = 0; op <= NDMA; ++op) memop(op, 0, 0);
  STAMP(3);
#pragma unroll
  for (int op = 0; op <= NDMA; ++op) memop(op, 1, K1_STAGE);
  STAMP(4);

  int xrd0[4], xrd1[4];
#pragma unroll
  for (int i = 0; i < 4; ++i) {
    const int rb = (mw * 64 + ((i + nw) & 3) * 16 + l15) * 128;
    xrd0[i] = rb + ((g ^ (lane & 7)) << 4);
    xrd1[i] = rb + (((4 + g) ^ (lane & 7)) << 4);
  }
  const int ga = g >> 1, gb = g & 1, dq = (lane >> 2) & 3, p = lane & 3;
  const int rho0 = 16 * ga + 4 * gb + dq;
  int wrd[NTW], tsl[NTW];
  unsigned wmask[NTW];
#pragma unroll
  for (int t = 0; t < NTW; ++t) {
    int T;
    if (QKV) {
      const int kind = t ^ (2 * mw);
      T = kind < 3 ? 5 * kind + nw : 5 * nw + 4;
    } else {
      T = nw * 3 + (t + 2 * mw) % 3;
    }
    tsl[t] = T;
    int ch = 2 * T + (p >> 1);
    if (!QKV) ch ^= 2 * ((rho0 >> 1) & 3);
    wrd[t] = K1_XB + rho0 * RS + ch * 16 + (p & 1) * 8;
    wmask[t] = ~0u;
    if (QKV && (t & 1) && T == 19) {
      wrd[t] = K1_ONES;
      wmask[t] = 0u;
    }
  }
  if (QKV && tid < 4) *(LAS unsigned*)(smem + K1_ONES + tid * 4) = 0x3f803f80u;

  f32x4 acc[4][NTW];
#pragma unroll
  for (int i = 0; i < 4; ++i)
#pragma unroll
    for (int t = 0; t < NTW; ++t) acc[i][t] = (f32x4){0.f, 0.f, 0.f, 0.f};
  f32x4 gram = {0.f, 0.f, 0.f, 0.f}, rsum = {0.f, 0.f, 0.f, 0.f};
  f32x4 cm0 = {0.f, 0.f, 0.f, 0.f}, cm1 = {0.f, 0.f, 0.f, 0.f};
  const unsigned ones = 0x3f803f80u;
  u32x4 onesu = {ones, ones, ones, ones};
  asm volatile("" : "+v"(onesu));
  const bf16x8 onesv = __builtin_bit_cast(bf16x8, onesu);

  bf16x8 xf0[4], wf0[NTW], xf1[4], wf1[NTW];
  auto rdw = [&](bf16x8(&wf)[NTW], unsigned sbo, int kk, int t) {
    if (t >= NTW) return;
    if (QKV && (t & 1)) {
      const unsigned o0 = (sbo + kk * 32 * RS) & wmask[t], o1 = (sbo + kk * 32 * RS + 8 * RS) & wmask[t];
      wf[t] = tr_frag(smem + wrd[t] + o0, smem + wrd[t] + o1);
    } else {
      wf[t] = tr_frag(smem + sbo + wrd[t] + kk * 32 * RS, smem + sbo + wrd[t] + kk * 32 * RS + 8 * RS);
    }
  };
  auto rdx = [&](bf16x8(&xf)[4], unsigned sbo, int kk, int i) {
    xf[i] = *(const LAS bf16x8*)(smem + sbo + (kk ? xrd1[i] : xrd0[i]));
  };
  auto compute = [&](bf16x8(&xf)[4], bf16x8(&wf)[NTW], bf16x8(&xn)[4], bf16x8(&wn)[NTW], unsigned sb, int kk,
                     int op0, int sd, unsigned bufoff) {
#pragma unroll
    for (int t = 0; t < 3; ++t) {
      acc[0][t] = mfma16(wf[t], xf[0], acc[0][t]);
      acc[1][t] = mfma16(wf[t], xf[1], acc[1][t]);
      SB();
      rdw(wn, sb, kk, t);
      if (t < 2) {
        rdx(xn, sb, kk, 2 * t);
        rdx(xn, sb, kk, 2 * t + 1);
      } else {
        rdw(wn, sb, kk, 3);
      }
      SB();
      acc[2][t] = mfma16(wf[t], xf[2], acc[2][t]);
      acc[3][t] = mfma16(wf[t], xf[3], acc[3][t]);
      if (t == 0) gram = mfma16(xf[0], xf[0], gram);
      SB();
      memop(op0 + t, sd, bufoff);
      SB();
    }
    if (QKV) {
#pragma unroll
      for (int i = 0; i < 4; ++i) acc[i][NTW - 1] = mfma16(wf[NTW - 1], xf[i], acc[i][NTW - 1]);
    } else {
      rsum = mfma16(onesv, xf[0], rsum);
    }
    cm0 = mfma16(wf[0], onesv, cm0);
    cm1 = mfma16(wf[1], onesv, cm1);
    SB();
    memop(op0 + 3, sd, bufoff);
    SB();
  };

  asm volatile("s_waitcnt vmcnt(%0) lgkmcnt(0)\n\ts_barrier" ::"n"(NDMA + 2) : "memory");
  STAMP(1);
#pragma unroll
  for (int t = 0; t < 4; ++t) {
    rdw(wf0, 0u, 0, t);
    rdx(xf0, 0u, 0, t);
  }
#pragma unroll
  for (int op = 0; op < 4; ++op) memop(op, 2, 2 * K1_STAGE);
  unsigned bufc = 0, bufo = 2 * K1_STAGE;
#pragma unroll 1
  for (int s = 0; s < 10; ++s) {
    compute(xf0, wf0, xf1, wf1, bufc, 1, 4, s + 2, bufo);
    bufo = bufc;
    bufc = bufc == 2 * K1_STAGE ? 0 : bufc + K1_STAGE;
    __builtin_amdgcn_s_waitcnt(0xC07F);
    asm volatile("s_waitcnt vmcnt(%0)\n\ts_barrier" ::"n"(NDMA + 2) : "memory");
    compute(xf1, wf1, xf0, wf0, bufc, 0, 0, s + 3, bufo);
  }
  STAMP(11);

  LAS float* st_sum = (LAS float*)(smem + K1_ST);
  LAS float* st_sq = st_sum + 128;
  LAS float* st_cs = st_sum + 256;
  {
    const int r = l15 & 3;
    const float dg = r == 0 ? gram[0] : r == 1 ? gram[1] : r == 2 ? gram[2] : gram[3];
    if (g == (l15 >> 2)) st_sq[(4 * mw + nw) * 16 + l15] = dg;
    if (l15 == 0) {
      *(LAS f32x4*)(st_cs + tsl[0] * 16 + 4 * g) = cm0;
      *(LAS f32x4*)(st_cs + tsl[1] * 16 + 4 * g) = cm1;
    }
    if (QKV) {
      if (mw == 1) {
        asm volatile("" ::: "memory");
#pragma unroll
        for (int i = 0; i < 4; ++i) {
          const f32x4 t0 = acc[i][0], t1 = acc[i][1];
          acc[i][0] = acc[i][2];
          acc[i][1] = acc[i][NTW - 1];
          acc[i][2] = t0;
          acc[i][NTW - 1] = t1;
        }
      }
      if (g == 0 && nw == 3) {
#pragma unroll
        for (int i = 0; i < 4; ++i) st_sum[(4 * mw + ((i + nw) & 3)) * 16 + l15] = acc[i][NTW - 1][0];
      }
    } else {
      if (g == 0) st_sum[(4 * mw + nw) * 16 + l15] = rsum[0];
    }
  }
  asm volatile("s_waitcnt vmcnt(0) lgkmcnt(0)\n\ts_barrier" ::: "memory");
  float mr[4], rstd[4];
#pragma unroll
  for (int i = 0; i < 4; ++i) {
    const int row = (4 * mw + ((i + nw) & 3)) * 16 + l15;
    const float mean = st_sum[row] * (1.f / 640.f);
    float var = __builtin_fmaf(st_sq[row], 1.f / 640.f, -mean * mean);
    var = var > 0.f ? var : 0.f;
    float r = __builtin_amdgcn_rsqf(var + 1e-5f);
    if (!QKV) r *= __builtin_amdgcn_rsqf(var * r * r + 1e-5f);
    rstd[i] = r;
    mr[i] = -mean * r;
  }
#define STAUX 16
  auto store_pair = [&](int i, int ubase, int Tg, u32x2 a, u32x2 b) {
    const auto s0 = __builtin_amdgcn_permlane16_swap(a[0], b[0], false, false);
    const auto s1 = __builtin_amdgcn_permlane16_swap(a[1], b[1], false, false);
    const u32x4 o = {s0[0], s1[0], s0[1], s1[1]};
    const int m16 = mtile * 8 + mw * 4 + ((i + (g & 1) + nw) & 3);
    const unsigned voff = afrag(m16, 0) + ((g >> 1) * 16 + l15) * 16 + (unsigned)OFF_AF + (ubase + (Tg >> 1)) * 4096 + (Tg & 1) * 512;
    __builtin_amdgcn_raw_buffer_store_b128(o, rws, voff, 0, STAUX);
  };
  STAMP(12);

  if (!QKV) {
#pragma unroll
    for (int t = 0; t < NTW; ++t) {
      const int Tg = grp * 12 + tsl[t];
      const f32x4 cs = *(const LAS f32x4*)(st_cs + tsl[t] * 16 + 4 * g);
      const float bv[4] = {bflo(b1r[t][0]), bfhi(b1r[t][0]), bflo(b1r[t][1]), bfhi(b1r[t][1])};
      u32x2 o[4];
#pragma unroll
      for (int i = 0; i < 4; ++i) {
        float hv[4];
#pragma unroll
        for (int r = 0; r < 4; ++r)
          hv[r] = gelu_tanh(__builtin_fmaf(acc[i][t][r], rstd[i], __builtin_fmaf(mr[i], cs[r], bv[r])));
        o[i] = (u32x2){pack2(hv[0], hv[1]), pack2(hv[2], hv[3])};
        if (i & 1) store_pair(i - 1, 0, Tg, o[i - 1], o[i]);
      }
    }
    if (job < 960) {
      const int u = job / 20, nb = job - u * 20;
      u32x4 lo, hi;
#pragma unroll
      for (int j = 0; j < 4; ++j) {
        lo[j] = __builtin_amdgcn_perm(w2r[2 * j + 1], w2r[2 * j], 0x05040100u);
        hi[j] = __builtin_amdgcn_perm(w2r[2 * j + 1], w2r[2 * j], 0x07060302u);
      }
      const int n16 = nb * 2 + (l15 >> 3), nl = (2 * l15) & 15;
      const unsigned dst = (unsigned)OFF_BF + bfrag(n16, u) + (g * 16 + nl) * 16;
      __builtin_amdgcn_raw_buffer_store_b128(lo, rws, dst, 0, 0);
      __builtin_amdgcn_raw_buffer_store_b128(hi, rws, dst + 16, 0, 0);
    }
  } else {
    constexpr int PVS = 352;
    auto tile = [&](int t, int T, bool isphi, bool isq) {
      const f32x4 cs = *(const LAS f32x4*)(st_cs + T * 16 + 4 * g);
      u32x2 o[4];
#pragma unroll
      for (int i = 0; i < 4; ++i) {
        float v[4];
#pragma unroll
        for (int r = 0; r < 4; ++r) {
          if (isphi) {
            const float tp = __builtin_fmaf(acc[i][t][r], rstd[i], __builtin_fmaf(mr[i], cs[r], 1e-6f));
            const float e = __builtin_amdgcn_exp2f(__builtin_fmaf(tp, 1.4426950408889634f, -1.4426950408889634e-6f));
            v[r] = __builtin_amdgcn_fmed3f(e, 0.f, 1.f) + __builtin_fmaxf(tp, 1e-6f);
          } else {
            v[r] = __builtin_fmaf(acc[i][t][r], rstd[i], mr[i] * cs[r]);
          }
        }
        o[i] = (u32x2){pack2(v[0], v[1]), pack2(v[2], v[3])};
        if (isq) {
          if (i & 1) store_pair(i - 1, 48, grp * 5 + T, o[i - 1], o[i]);
        } else {
          *(LAS u32x2*)(smem + (mw * 64 + ((i + nw) & 3) * 16 + l15) * PVS + (T - 5) * 32 + g * 8) = o[i];
        }
      }
    };
    tile(0, nw, true, true);
    if (nw == 0) tile(3, 4, true, true);
    tile(1, 5 + nw, true, false);
    tile(2, 10 + nw, false, false);
    if (nw == 1) tile(3, 9, true, false);
    if (nw == 2) tile(3, 14, false, false);
    asm volatile("s_waitcnt lgkmcnt(0)\n\ts_barrier" ::: "memory");
    if (wid < 5) {
      const int dt = wid;
      const int trow = (4 * g + dq) * PVS + (p >> 1) * 16 + (p & 1) * 8;
      f32x4 kvacc[3], zacc = {0.f, 0.f, 0.f, 0.f};
#pragma unroll
      for (int j = 0; j < 3; ++j) kvacc[j] = (f32x4){0.f, 0.f, 0.f, 0.f};
#pragma unroll
      for (int ks = 0; ks < 4; ++ks) {
        const LAS char* pb = smem + ks * 32 * PVS + trow;
        const bf16x8 a = tr_frag(pb + dt * 32, pb + dt * 32 + 16 * PVS);
        zacc = mfma16(a, onesv, zacc);
#pragma unroll
        for (int j = 0; j < 3; ++j) {
          int et = dt - 1 + j;
          et = et < 0 ? 0 : (et > 4 ? 4 : et);
          const bf16x8 b = tr_frag(pb + 160 + et * 32, pb + 160 + et * 32 + 16 * PVS);
          kvacc[j] = mfma16(a, b, kvacc[j]);
        }
      }
      const int dl = dt * 16 + 4 * g, hd = dl / 20, dg = (dl - hd * 20) >> 2;
      const unsigned kvp = (unsigned)OFF_KVP + (unsigned)(((grp * 4 + hd) * 16 + mtile) * 1600);
#pragma unroll
      for (int j = 0; j < 3; ++j) {
        const int e = (dt - 1 + j) * 16 + l15 - hd * 20;
        if (e >= 0 && e < 20)
          __builtin_amdgcn_raw_buffer_store_b128(__builtin_bit_cast(u32x4, kvacc[j]), rws, kvp + (dg * 20 + e) * 16, 0, 0);
      }
      if (l15 == 0)
        __builtin_amdgcn_raw_buffer_store_b128(__builtin_bit_cast(u32x4, zacc), rws,
                                               (unsigned)OFF_ZP + (mtile * 640 + grp * 80 + dl) * 4, 0, 0);
    }
  }
  STAMP(13);
  STAMP(14);
}

__global__ void __launch_bounds__(512, 2)
k1_kernel(const __hip_bfloat16* __restrict__ x, const __hip_bfloat16* __restrict__ wqkv,
          const __hip_bfloat16* __restrict__ w1, const __hip_bfloat16* __restrict__ b1,
          const __hip_bfloat16* __restrict__ w2, char* __restrict__ ws) {
  extern __shared__ __attribute__((aligned(16))) char smem_[];
  LAS char* smem = (LAS char*)smem_;
  const int b = blockIdx.x, xcd = b & 7, idx = b >> 3;
  const int mtile = (xcd & 3) * 4 + (idx & 3), sel = idx >> 2;
  const int grp = (xcd >> 2) * 4 + (sel & 3);
  if (threadIdx.x < 200) {
    const int c = b * 200 + threadIdx.x, n80 = c / 6400, r = c - n80 * 6400;
    *(u32x4*)(ws + OFF_BF + (size_t)(n80 * NU + 48) * 5120 + r * 16) = (u32x4){~0u, ~0u, ~0u, ~0u};
  }
  if (sel >> 2)
    k1_body<false>(x, w1, b1, w2, ws, mtile, grp, sel, smem);
  else
    k1_body<true>(x, wqkv, b1, w2, ws, mtile, grp, sel, smem);
}

constexpr int K2_LDS = 163840;
constexpr int K2_RED = 0;
constexpr int K2_KVT = 4 * 8192;
constexpr int K2_WOS = 5 * 8192;
constexpr int K2_ZB = 6 * 8192;
constexpr int K2_CNT = 7 * 8192;

__device__ __forceinline__ unsigned helper_arrive(LAS unsigned* c, int lane) {
  unsigned old = 0;
  __builtin_amdgcn_s_waitcnt(0xC07F);
  if (lane == 0) old = __hip_atomic_fetch_add(c, 1u, __ATOMIC_RELAXED, __HIP_MEMORY_SCOPE_WORKGROUP);
  return __builtin_amdgcn_readfirstlane(old);
}
__device__ __forceinline__ void helper_wait(LAS unsigned* c) {
  while (__builtin_amdgcn_readfirstlane(__hip_atomic_load(c, __ATOMIC_RELAXED, __HIP_MEMORY_SCOPE_WORKGROUP)) < 4u)
    __builtin_amdgcn_s_sleep(1);
  asm volatile("" ::: "memory");
}

__global__ void __launch_bounds__(512, 2)
k2_kernel(const __hip_bfloat16* __restrict__ x, const __hip_bfloat16* __restrict__ wo,
          const __hip_bfloat16* __restrict__ b2, char* __restrict__ ws, __hip_bfloat16* __restrict__ out) {
  extern __shared__ __attribute__((aligned(16))) char smem_[];
  LAS char* smem = (LAS char*)smem_;
  const int tid = threadIdx.x, lane = tid & 63;
  const int wid = __builtin_amdgcn_readfirstlane(tid >> 6);
  const int w = wid & 3, helper = wid >> 2;
  const int l15 = lane & 15, g = lane >> 4;
  const int b = blockIdx.x, xcd = b & 7, idx = b >> 3;
  const int mt = (xcd & 3) * 8 + (idx & 7);
  const int nt = (xcd >> 2) * 4 + (idx >> 3);
  const int ph = xcd * 4 + (idx & 3), ps = idx >> 2;
  constexpr int PKID = 1;
  STAMP(0);

  const __amdgpu_buffer_rsrc_t rab = __builtin_amdgcn_make_buffer_rsrc(ws + OFF_AF, 0, 168 * NU * 1024, 0x00020000);
  const unsigned aoff = (unsigned)(mt * 4 * NU * 1024 + lane * 16);
  const unsigned boff = (unsigned)((128 + nt * 5) * NU * 1024 + lane * 16);
  const int u0 = w * 12, uo = 48 + w * 5;
  LAS unsigned* cnt = (LAS unsigned*)(smem + K2_CNT);
  const int token = mt * 64 + w * 16 + l15;
  u32x2 xr[3], br[3];
  auto load_resid = [&]() {
#pragma unroll
    for (int j = 0; j < 3; ++j) {
      const int jj = helper * 3 + j < 5 ? helper * 3 + j : 4;
      xr[j] = *(const u32x2*)(x + (size_t)token * D_ + nt * 80 + jj * 16 + 4 * g);
      br[j] = *(const u32x2*)(b2 + nt * 80 + jj * 16 + 4 * g);
    }
  };

  if (helper == 0) {
    f32x4 acc[4][5];
#pragma unroll
    for (int i = 0; i < 4; ++i)
#pragma unroll
      for (int j = 0; j < 5; ++j) acc[i][j] = (f32x4){0.f, 0.f, 0.f, 0.f};
    u32x4 fa[4][4], fb[4][5];
    auto load_one = [&](int s, int n) {
      const int slot = s & 3;
      if (s < 12) {
        if (n < 5)
          fb[slot][n] = __builtin_amdgcn_raw_buffer_load_b128(rab, boff + n * 1024, (u0 + s) * 5120, 0);
        else
          fa[slot][n - 5] = __builtin_amdgcn_raw_buffer_load_b128(rab, aoff + (n - 5) * 1024, (u0 + s) * 4096, 0);
      }
    };
    asm volatile("s_barrier" ::: "memory");
#pragma unroll
    for (int k = 0; k < 3; ++k)
#pragma unroll
      for (int n = 0; n < 9; ++n) load_one(k, n);
    SB();
    STAMP(11);
    auto step = [&](auto sc_) {
      constexpr int s = decltype(sc_)::value;
      constexpr int slot = s & 3;
#pragma unroll
      for (int q = 0; q < 10; ++q) {
#pragma unroll
        for (int h2 = 0; h2 < 2; ++h2) {
          const int i = (2 * q + h2) / 5, j = (2 * q + h2) % 5;
          acc[i][j] = mfma16(__builtin_bit_cast(bf16x8, fb[slot][j]), __builtin_bit_cast(bf16x8, fa[slot][i]), acc[i][j]);
        }
        SB();
        if (q < 9) load_one(s + 3, q);
        SB();
      }
      if constexpr (s == 0) STAMP(13);
      if constexpr (s == 8) load_resid();
    };
    static_for<0, 12>(step);
    SB();
    STAMP(4);
    asm volatile("s_barrier" ::: "memory");
    STAMP(6);
#pragma unroll
    for (int i = 0; i < 4; ++i)
#pragma unroll
      for (int j = 0; j < 5; ++j) *(LAS f32x4*)(smem + (((i * 5 + j) * 8 + wid) * 64 + lane) * 16) = acc[i][j];
  } else {
    const int ht = tid - 256;
    __builtin_amdgcn_s_setprio(3);
    LAS float* kvt = (LAS float*)(smem + K2_KVT);
    LAS char* wos = smem + K2_WOS;
    f32x4 kvl[4][2];
    {
      const char* src = ws + OFF_KVP + (size_t)(ph * 16 + w * 4) * 1600;
#pragma unroll
      for (int pidx = 0; pidx < 4; ++pidx) {
        kvl[pidx][0] = *(const f32x4*)(src + pidx * 1600 + lane * 16);
        kvl[pidx][1] = *(const f32x4*)(src + pidx * 1600 + (lane < 36 ? lane + 64 : 99) * 16);
      }
    }
    const int tc = ht < 200 ? ht : 199;
    const int pg = tc / 40, pn = tc - pg * 40;
    const u32x4 wol = *(const u32x4*)(wo + (size_t)(ph * 20 + tc / 10) * D_ + ps * 80 + (tc % 10) * 8);
    if (ht < 4) cnt[ht] = 0u;
    SB();
    asm volatile("s_waitcnt lgkmcnt(0)\n\ts_barrier" ::: "memory");
    *(LAS f32x4*)(smem + K2_RED + w * 8192 + lane * 16) = (kvl[0][0] + kvl[1][0]) + (kvl[2][0] + kvl[3][0]);
    *(LAS f32x4*)(smem + K2_RED + w * 8192 + (64 + lane) * 16) = (kvl[0][1] + kvl[1][1]) + (kvl[2][1] + kvl[3][1]);
    *(LAS u32x4*)(wos + tc * 16) = wol;
    helper_arrive(cnt, lane);
    helper_wait(cnt);
    if (w < 2) {
      const LAS char* r = smem + K2_RED + ht * 16;
      const f32x4 t = (*(const LAS f32x4*)r + *(const LAS f32x4*)(r + 8192)) +
                      (*(const LAS f32x4*)(r + 16384) + *(const LAS f32x4*)(r + 24576));
      const u32x2 pk = {pack2(t[0], t[1]), pack2(t[2], t[3])};
      const int idg = ht / 20, ie = ht - idg * 20;
      if (ht < 100) *(LAS f32x4*)(kvt + ie * 20 + idg * 4) = (f32x4){bflo(pk[0]), bfhi(pk[0]), bflo(pk[1]), bfhi(pk[1])};
    }
    helper_arrive(cnt + 1, lane);
    helper_wait(cnt + 1);
    {
      f32x4 o0 = {0.f, 0.f, 0.f, 0.f}, o1 = {0.f, 0.f, 0.f, 0.f};
#pragma unroll
      for (int e = 0; e < 20; ++e) {
        const f32x4 kq = *(const LAS f32x4*)(kvt + e * 20 + pg * 4);
        const unsigned wv = *(const LAS unsigned*)(wos + e * 160 + pn * 4);
        o0 += kq * bflo(wv);
        o1 += kq * bfhi(wv);
      }
      const int q = 5 * ph + pg, n = ps * 80 + 2 * pn;
      char* dst = ws + OFF_BF + bfrag(n >> 4, 48 + (q >> 3)) + (((q >> 1) & 3) * 16 + (n & 15)) * 16 + (q & 1) * 8;
      const u32x2 v0 = {pack2(o0[0], o0[1]), pack2(o0[2], o0[3])}, v1 = {pack2(o1[0], o1[1]), pack2(o1[2], o1[3])};
      asm volatile("global_store_dwordx2 %0, %1, off sc1\n\tglobal_store_dwordx2 %0, %2, off offset:16 sc1"
                   ::"v"(dst), "v"(v0), "v"(v1) : "memory");
    }
    f32x4 zl[16];
    {
      const char* src = ws + OFF_ZP + (w * 40 + (lane < 40 ? lane : 39)) * 16;
#pragma unroll
      for (int pidx = 0; pidx < 16; ++pidx) zl[pidx] = *(const f32x4*)(src + pidx * 2560);
    }
    u32x4 pq[5][4];
#pragma unroll
    for (int ul = 0; ul < 5; ++ul)
#pragma unroll
      for (int i = 0; i < 4; ++i)
        pq[ul][i] = __builtin_amdgcn_raw_buffer_load_b128(rab, aoff + i * 1024, (uo + ul) * 4096, 0);
    SB();
    {
      f32x4 zs = zl[0];
#pragma unroll
      for (int pidx = 1; pidx < 16; ++pidx) zs += zl[pidx];
      if (lane < 40) *(LAS u32x2*)(smem + K2_ZB + w * 320 + lane * 8) = (u32x2){pack2(zs[0], zs[1]), pack2(zs[2], zs[3])};
    }
    u32x4 zp[5];
#pragma unroll
    for (int ul = 0; ul < 5; ++ul) zp[ul] = *(const LAS u32x4*)(smem + K2_ZB + w * 320 + (32 * ul + 8 * g) * 2);
    u32x4 fb[3][5];
    auto load_wp = [&](int ul) {
#pragma unroll
      for (int j = 0; j < 5; ++j)
        fb[ul % 3][j] = __builtin_amdgcn_raw_buffer_load_b128(rab, boff + j * 1024, (uo + ul) * 5120, 16);
    };
    load_wp(0);
    load_wp(1);
    load_wp(2);
    SB();
#pragma unroll
    for (int ih = 0; ih < 2; ++ih) {
      float rden[2][12];
#pragma unroll
      for (int c = 0; c < 3; ++c) {
        const int tau = 4 * c + (l15 & 3);
        const int htar = (32 * (tau >> 1) + 8 * (l15 >> 2) + 4 * (tau & 1)) / 20;
        f32x4 dacc[2];
#pragma unroll
        for (int i = 0; i < 2; ++i) dacc[i] = (f32x4){1e-6f, 1e-6f, 1e-6f, 1e-6f};
#pragma unroll
        for (int ul = 0; ul < 5; ++ul) {
          if (c == 0 && ul > 2) continue;
          if (c == 1 && ul < 1) continue;
          if (c == 2 && ul < 3) continue;
          u32x4 zf;
#pragma unroll
          for (int i = 0; i < 2; ++i) {
            const bool m = (32 * ul + 8 * g + 4 * i) / 20 == htar;
            zf[2 * i] = m ? zp[ul][2 * i] : 0u;
            zf[2 * i + 1] = m ? zp[ul][2 * i + 1] : 0u;
          }
#pragma unroll
          for (int i = 0; i < 2; ++i)
            dacc[i] = mfma16(__builtin_bit_cast(bf16x8, zf), __builtin_bit_cast(bf16x8, pq[ul][2 * ih + i]), dacc[i]);
        }
#pragma unroll
        for (int i = 0; i < 2; ++i)
#pragma unroll
          for (int r = 0; r < 4; ++r)
            if (4 * c + r < 10) rden[i][4 * c + r] = __builtin_amdgcn_rcpf(dacc[i][r]);
      }
#pragma unroll
      for (int ul = 0; ul < 5; ++ul)
#pragma unroll
        for (int i = 0; i < 2; ++i) {
#pragma unroll
          for (int j = 0; j < 4; ++j) {
            const float sc = rden[i][2 * ul + (j >> 1)];
            pq[ul][2 * ih + i][j] = pack2(bflo(pq[ul][2 * ih + i][j]) * sc, bfhi(pq[ul][2 * ih + i][j]) * sc);
          }
          asm volatile("" : "+v"(pq[ul][2 * ih + i]));
        }
      SB();
    }
    f32x4 acc[4][5];
#pragma unroll
    for (int i = 0; i < 4; ++i)
#pragma unroll
      for (int j = 0; j < 5; ++j) acc[i][j] = (f32x4){0.f, 0.f, 0.f, 0.f};
    auto ostep = [&](auto sc_) {
      constexpr int ul = decltype(sc_)::value;
      auto missing = [&]() {
        unsigned m = 0;
#pragma unroll
        for (int j = 0; j < 5; ++j) m |= (fb[ul % 3][j][0] == ~0u) | (fb[ul % 3][j][2] == ~0u);
        return __any(m);
      };
      if (__builtin_expect(missing(), 0)) {
        unsigned spins = 0;
        do {
          __builtin_amdgcn_s_sleep(2);
          load_wp(ul);
        } while (missing() && ++spins < (1u << 20));
      }
#pragma unroll
      for (int i = 0; i < 4; ++i)
#pragma unroll
        for (int j = 0; j < 5; ++j)
          acc[i][j] = mfma16(__builtin_bit_cast(bf16x8, fb[ul % 3][j]), __builtin_bit_cast(bf16x8, pq[ul][i]), acc[i][j]);
      SB();
      if constexpr (ul + 3 < 5) load_wp(ul + 3);
      if constexpr (ul == 2) load_resid();
      SB();
    };
    static_for<0, 5>(ostep);
#pragma unroll
    for (int i = 0; i < 4; ++i)
#pragma unroll
      for (int j = 0; j < 5; ++j) *(LAS f32x4*)(smem + (((i * 5 + j) * 8 + wid) * 64 + lane) * 16) = acc[i][j];
    asm volatile("s_barrier" ::: "memory");
  }

  asm volatile("s_waitcnt lgkmcnt(0)\n\ts_barrier" ::: "memory");
#pragma unroll
  for (int j = 0; j < 3; ++j) {
    if (j == 2 && helper) break;
    const int jj = helper * 3 + j;
    f32x4 fin = *(const LAS f32x4*)(smem + (((w * 5 + jj) * 8) * 64 + lane) * 16);
#pragma unroll
    for (int sidx = 1; sidx < 8; ++sidx) fin += *(const LAS f32x4*)(smem + (((w * 5 + jj) * 8 + sidx) * 64 + lane) * 16);
    const float o0 = fin[0] + bflo(xr[j][0]) + bflo(br[j][0]);
    const float o1 = fin[1] + bfhi(xr[j][0]) + bfhi(br[j][0]);
    const float o2 = fin[2] + bflo(xr[j][1]) + bflo(br[j][1]);
    const float o3 = fin[3] + bfhi(xr[j][1]) + bfhi(br[j][1]);
    *(u32x2*)(out + (size_t)token * D_ + nt * 80 + jj * 16 + 4 * g) = (u32x2){pack2(o0, o1), pack2(o2, o3)};
  }
  STAMP(7);
  STAMP(8);
}

}

extern "C" void kernel_launch(void* const* d_in, const int* in_sizes, int n_in, void* d_out, int out_size, void* d_ws,
                              size_t ws_size, hipStream_t stream) {
  const __hip_bfloat16* x = (const __hip_bfloat16*)d_in[0];
  const __hip_bfloat16* wqkv = (const __hip_bfloat16*)d_in[1];
  const __hip_bfloat16* wo = (const __hip_bfloat16*)d_in[2];
  const __hip_bfloat16* w1 = (const __hip_bfloat16*)d_in[4];
  const __hip_bfloat16* b1 = (const __hip_bfloat16*)d_in[5];
  const __hip_bfloat16* w2 = (const __hip_bfloat16*)d_in[6];
  const __hip_bfloat16* b2 = (const __hip_bfloat16*)d_in[7];
  (void)hipFuncSetAttribute((const void*)k1_kernel, hipFuncAttributeMaxDynamicSharedMemorySize, K1_LDS);
  (void)hipFuncSetAttribute((const void*)k2_kernel, hipFuncAttributeMaxDynamicSharedMemorySize, K2_LDS);
  k1_kernel<<<256, 512, K1_LDS, stream>>>(x, wqkv, w1, b1, w2, (char*)d_ws);
  k2_kernel<<<256, 512, K2_LDS, stream>>>(x, wo, b2, (char*)d_ws, (__hip_bfloat16*)d_out);
}
```

```cpp
#include <hip/hip_runtime.h>
#include <hip/hip_bf16.h>
#include <stdint.h>

typedef __attribute__((ext_vector_type(8))) __bf16 bf16x8;
typedef __attribute__((ext_vector_type(4))) __bf16 bf16x4;
typedef __attribute__((ext_vector_type(2))) __bf16 bf16x2;
typedef __attribute__((ext_vector_type(4))) float f32x4;
typedef __attribute__((ext_vector_type(2))) float f32x2;
typedef __attribute__((ext_vector_type(4))) unsigned u32x4;
typedef __attribute__((ext_vector_type(2))) unsigned u32x2;
#define LAS __attribute__((address_space(3)))
#define GAS __attribute__((address_space(1)))
typedef GAS unsigned gu32;
typedef GAS unsigned long long gu64;
#define SB() __builtin_amdgcn_sched_barrier(0)

namespace {

constexpr int S_ = 2048, D_ = 640, F_ = 1536;
constexpr int NU = 68;
constexpr size_t OFF_AF = 0;
constexpr size_t OFF_BF = OFF_AF + (size_t)128 * NU * 1024;
constexpr size_t OFF_KVP = OFF_BF + (size_t)40 * NU * 1024;
constexpr size_t OFF_ZP = OFF_KVP + (size_t)32 * 16 * 1600;
constexpr size_t WS_BYTES = OFF_ZP + (size_t)16 * 640 * 4;

constexpr int K1_XB = 16384;
constexpr int K1_STAGE = K1_XB + 30720;
constexpr int K1_PF = 3 * K1_STAGE;
constexpr int K1_ST = K1_PF + 2048;
constexpr int K1_ONES = K1_ST + 2560;
constexpr int K1_DUMMY = K1_ONES + 16;
constexpr int K1_LDS = K1_DUMMY + 1024;

__device__ __forceinline__ unsigned afrag(int m16, int u) { return (unsigned)((((m16 >> 2) * NU + u) * 4 + (m16 & 3)) * 1024); }
__device__ __forceinline__ unsigned bfrag(int n16, int u) {
  const int n80 = n16 / 5;
  return (unsigned)(((n80 * NU + u) * 5 + (n16 - n80 * 5)) * 1024);
}

template <int V>
struct IntC {
  static constexpr int value = V;
};
template <int B, int E, class F>
__device__ __forceinline__ void static_for(F&& f) {
  if constexpr (B < E) {
    f(IntC<B>{});
    static_for<B + 1, E>(f);
  }
}

__device__ __forceinline__ u32x4 make_rsrc(const void* p, unsigned bytes) {
  const unsigned long long a = (unsigned long long)p;
  u32x4 r;
  r[0] = (unsigned)a;
  r[1] = (unsigned)(a >> 32) & 0xffffu;
  r[2] = bytes;
  r[3] = 0x00020000u;
  return r;
}

__device__ __forceinline__ void dma16(u32x4 rsrc, unsigned voff, unsigned soff, unsigned lds) {
  unsigned keep;
  asm volatile(
      "s_mov_b32 %0, m0\n\t"
      "s_mov_b32 m0, %4\n\t"
      "s_nop 0\n\t"
      "buffer_load_dwordx4 %1, %2, %3 offen lds\n\t"
      "s_mov_b32 m0, %0"
      : "=&s"(keep)
      : "v"(voff), "s"(rsrc), "s"(soff), "s"(lds)
      : "memory");
}

__device__ __forceinline__ void dma4(u32x4 rsrc, unsigned voff, unsigned soff, unsigned lds) {
  unsigned keep;
  asm volatile(
      "s_mov_b32 %0, m0\n\t"
      "s_mov_b32 m0, %4\n\t"
      "s_nop 0\n\t"
      "buffer_load_dword %1, %2, %3 offen lds\n\t"
      "s_mov_b32 m0, %0"
      : "=&s"(keep)
      : "v"(voff), "s"(rsrc), "s"(soff), "s"(lds)
      : "memory");
}

__device__ __forceinline__ void warm_code(unsigned long long base, unsigned len, unsigned off, int lane, unsigned lds) {
  const u32x4 r = make_rsrc((const void*)(base & ~127ull), len);
  dma4(r, off + lane * 128, 0, lds);
}
__device__ __forceinline__ unsigned long long k2_code_address() {
  unsigned long long a;
  asm volatile(
      "s_getpc_b64 vcc\n\t"
      "s_add_u32 vcc_lo, vcc_lo, _ZN12_GLOBAL__N_19k2_kernelEPK14__hip_bfloat16S2_S2_PcPS0_@rel32@lo+4\n\t"
      "s_addc_u32 vcc_hi, vcc_hi, _ZN12_GLOBAL__N_19k2_kernelEPK14__hip_bfloat16S2_S2_PcPS0_@rel32@hi+12\n\t"
      "s_mov_b64 %0, vcc"
      : "=s"(a)
      :
      : "scc", "vcc");
  return a;
}
__device__ __forceinline__ unsigned long long k1_code_address() {
  unsigned long long a;
  asm volatile(
      "s_getpc_b64 vcc\n\t"
      "s_add_u32 vcc_lo, vcc_lo, _ZN12_GLOBAL__N_19k1_kernelEPK14__hip_bfloat16S2_S2_S2_S2_Pc@rel32@lo+4\n\t"
      "s_addc_u32 vcc_hi, vcc_hi, _ZN12_GLOBAL__N_19k1_kernelEPK14__hip_bfloat16S2_S2_S2_S2_Pc@rel32@hi+12\n\t"
      "s_mov_b64 %0, vcc"
      : "=s"(a)
      :
      : "scc", "vcc");
  return a;
}

__device__ __forceinline__ unsigned pack2(float a, float b) {
  f32x2 p = {a, b};
  return __builtin_bit_cast(unsigned, __builtin_convertvector(p, bf16x2));
}
__device__ __forceinline__ float bflo(unsigned u) { return __builtin_bit_cast(float, u << 16); }
__device__ __forceinline__ float bfhi(unsigned u) { return __builtin_bit_cast(float, u & 0xffff0000u); }
__device__ __forceinline__ f32x4 mfma16(bf16x8 a, bf16x8 b, f32x4 c) {
  return __builtin_amdgcn_mfma_f32_16x16x32_bf16(a, b, c, 0, 0, 0);
}
__device__ __forceinline__ bf16x8 tr_frag(const LAS char* p0, const LAS char* p1) {
  bf16x4 lo = __builtin_amdgcn_ds_read_tr16_b64_v4bf16((LAS bf16x4*)p0);
  bf16x4 hi = __builtin_amdgcn_ds_read_tr16_b64_v4bf16((LAS bf16x4*)p1);
  return __builtin_shufflevector(lo, hi, 0, 1, 2, 3, 4, 5, 6, 7);
}
__device__ __forceinline__ float gelu_tanh(float x) {
  const float c1 = -2.f * 0.7978845608028654f * 1.4426950408889634f;
  const float c2 = c1 * 0.044715f;
  const float t = x * __builtin_fmaf(x * x, c2, c1);
  const float e = __builtin_amdgcn_exp2f(t);
  return x * __builtin_amdgcn_rcpf(1.f + e);
}

template <bool QT>
__device__ __forceinline__ void k1_body(const __hip_bfloat16* __restrict__ x, const __hip_bfloat16* __restrict__ wqkv,
                                        const __hip_bfloat16* __restrict__ w1, const __hip_bfloat16* __restrict__ b1,
                                        const __hip_bfloat16* __restrict__ w2, char* __restrict__ ws, int mtile, int grp,
                                        int sel, LAS char* smem) {
  constexpr int CPR = QT ? 26 : 24;
  constexpr int RS = CPR * 16;
  constexpr int LDW = QT ? 1920 : 1536;
  constexpr int QOFF = K1_XB + 24576;
  constexpr int NDMA = 6;
  constexpr unsigned DROP = 0x40000000u;

  const int tid = threadIdx.x, lane = tid & 63;
  const int wid = __builtin_amdgcn_readfirstlane(tid >> 6);
  const int mw = wid >> 2, nw = mw ? 3 - (wid & 3) : wid;
  const int l15 = lane & 15, g = lane >> 4;
  const unsigned lds0 = (unsigned)(size_t)smem;

  const __amdgpu_buffer_rsrc_t rws = __builtin_amdgcn_make_buffer_rsrc(ws, 0, (unsigned)WS_BYTES, 0x00020000);
  const u32x4 rx = make_rsrc(x, S_ * D_ * 2);
  const u32x4 rq = make_rsrc(wqkv, D_ * 1920 * 2);
  const u32x4 rw = QT ? rq : make_rsrc(w1, D_ * 1536 * 2);

  unsigned pfoff;
  {
    const int mi = mtile & 7;
    if (mw == 0) {
      pfoff = (unsigned)((mtile * 128 + 32 * sel + 8 * nw + (lane & 7)) * D_ * 2);
    } else {
      const int row = 8 * mi + 2 * nw + ((lane >> 4) & 1);
      int j = lane & 15;
      if (QT) {
        j = j > 8 ? 8 : j;
        const int part = j / 3, wi = j - part * 3;
        pfoff = (unsigned)(row * LDW * 2 + part * 1280 + grp * 160 + (wi == 2 ? 156 : wi * 128));
      } else {
        j = j > 2 ? 2 : j;
        pfoff = (unsigned)(row * LDW * 2 + grp * 384 + j * 128);
      }
    }
  }
  const u32x4 rpf = mw == 0 ? rx : rw;
  const unsigned pfstep = mw == 0 ? 128u : (unsigned)(64 * LDW * 2);
  const unsigned pflds = lds0 + K1_PF + wid * 256;
  auto prefetch = [&](int s) { dma4(rpf, pfoff, s < 10 ? (unsigned)s * pfstep : DROP, pflds); };
  prefetch(0);
  prefetch(1);

  unsigned xoff[2], woff[4];
#pragma unroll
  for (int j = 0; j < 2; ++j) {
    const int piece = 2 * wid + j, row = piece * 8 + (lane >> 3), ch = (lane & 7) ^ (row & 7);
    xoff[j] = (unsigned)(((mtile * 128 + row) * D_ + ch * 8) * 2);
  }
#pragma unroll
  for (int j = 0; j < 4; ++j) {
    if (!QT && j == 3) {
      const int rho = 16 * (wid & 3) + (lane >> 2), c = (lane & 3) ^ (2 * ((rho >> 2) & 1));
      const int kl = (rho & ~12) | ((rho & 4) << 1) | ((rho & 8) >> 1);
      woff[j] = (unsigned)((kl * 1920 + grp * 80 + 48 + c * 8) * 2);
      continue;
    }
    const int piece = wid + 8 * j < CPR ? wid + 8 * j : 0;
    const int ci = piece * 64 + lane, rho = ci / CPR, cp = ci - rho * CPR;
    const int kl = (rho & ~12) | ((rho & 4) << 1) | ((rho & 8) >> 1);
    int col;
    if (QT) {
      col = (cp < 10 ? 640 + cp * 8 : cp < 20 ? 1280 + (cp - 10) * 8 : (cp - 20) * 8) + grp * 80;
    } else {
      const int c = cp ^ (2 * ((rho >> 1) & 3));
      col = grp * 192 + c * 8;
    }
    woff[j] = (unsigned)((kl * LDW + col) * 2);
  }
  const bool has4 = QT ? wid < 2 : wid < 4;
  auto memop = [&](int op, int sd, unsigned bufoff) {
    if (op < 2) {
      dma16(rx, xoff[op], sd < 10 ? (unsigned)(sd * 128) : DROP, lds0 + bufoff + (2 * wid + op) * 1024);
    } else if (op < 5) {
      dma16(rw, woff[op - 2], (unsigned)(sd * 64 * LDW * 2), lds0 + bufoff + K1_XB + (wid + 8 * (op - 2)) * 1024);
    } else if (op == 5) {
      dma16(rq, woff[3], has4 ? (unsigned)(sd * 64 * 1920 * 2) : DROP,
            has4 ? lds0 + bufoff + K1_XB + (24 + wid) * 1024 : lds0 + K1_DUMMY);
    } else if (op == NDMA) {
      prefetch(sd + 2);
    }
  };
#pragma unroll
  for (int op = 0; op < NDMA; ++op) memop(op, 0, 0);
  prefetch(10);
#pragma unroll
  for (int op = 0; op < NDMA; ++op) memop(op, 1, K1_STAGE);
  prefetch(10);

  int xrd0[4], xrd1[4];
#pragma unroll
  for (int i = 0; i < 4; ++i) {
    const int rb = (mw * 64 + ((i + nw) & 3) * 16 + l15) * 128;
    xrd0[i] = rb + ((g ^ (lane & 7)) << 4);
    xrd1[i] = rb + (((4 + g) ^ (lane & 7)) << 4);
  }
  const int ga = g >> 1, gb = g & 1, dq = (lane >> 2) & 3, p = lane & 3;
  const int rho0 = 16 * ga + 4 * gb + dq;
  int wrd[4], tsl[4];
  unsigned w2mask = ~0u;
#pragma unroll
  for (int t = 0; t < 4; ++t) {
    int T;
    if (QT) T = t < 3 ? 5 * t + nw : 5 * nw + 4;
    else T = t < 3 ? nw * 3 + t : 12 + nw;
    tsl[t] = T;
    if (!QT && t == 3) {
      wrd[t] = QOFF + rho0 * 64 + ((2 * (nw & 1) + (p >> 1)) ^ (2 * ((rho0 >> 2) & 1))) * 16 + (p & 1) * 8;
    } else {
      int ch = 2 * T + (p >> 1);
      if (!QT) ch ^= 2 * ((rho0 >> 1) & 3);
      wrd[t] = K1_XB + rho0 * RS + ch * 16 + (p & 1) * 8;
    }
    if (QT && t == 2 && nw == 3) {
      wrd[t] = K1_ONES;
      w2mask = 0u;
    }
  }
  if (QT && tid < 4) *(LAS unsigned*)(smem + K1_ONES + tid * 4) = 0x3f803f80u;

  f32x4 acc[4][4];
#pragma unroll
  for (int i = 0; i < 4; ++i)
#pragma unroll
    for (int t = 0; t < 4; ++t) acc[i][t] = (f32x4){0.f, 0.f, 0.f, 0.f};
  f32x4 gram = {0.f, 0.f, 0.f, 0.f}, rsum = {0.f, 0.f, 0.f, 0.f};
  f32x4 cm0 = {0.f, 0.f, 0.f, 0.f}, cm1 = {0.f, 0.f, 0.f, 0.f};
  const unsigned ones = 0x3f803f80u;
  u32x4 onesu = {ones, ones, ones, ones};
  asm volatile("" : "+v"(onesu));
  const bf16x8 onesv = __builtin_bit_cast(bf16x8, onesu);

  asm volatile("s_waitcnt vmcnt(%0) lgkmcnt(0)\n\ts_barrier" ::"n"(NDMA + 2) : "memory");
  prefetch(2);
  prefetch(3);
  if (sel == 0) {
    if (wid < 4) warm_code(k1_code_address(), 25500, wid * 8192, lane, pflds);
    else if (wid < 6) warm_code(k2_code_address(), 15980, (wid - 4) * 8192, lane, pflds);
  }
  unsigned w2r[8];
  int job = (mtile * 8 + grp) * 8 + wid;
  u32x2 b1r[3];
  if (!QT) {
#pragma unroll
    for (int t = 0; t < 3; ++t) b1r[t] = *(const u32x2*)(b1 + (grp * 12 + nw * 3 + t) * 16 + 4 * g);
    const int jc = job < 960 ? job : 959;
    const int u = jc / 20, nb = jc - u * 20;
#pragma unroll
    for (int j = 0; j < 8; ++j) {
      const int k = 32 * u + 8 * g + j;
      w2r[j] = *(const unsigned*)(w2 + (size_t)k * D_ + nb * 32 + 2 * l15);
    }
  }
  SB();

  auto mainloop = [&](auto mw_, auto ns_) {
    constexpr int MW = decltype(mw_)::value, NS = decltype(ns_)::value;
    bf16x8 xf0[4], wf0[NS], xf1[4], wf1[NS];
    auto rdw = [&](bf16x8(&wf)[NS], unsigned sbo, int kk, int t) {
      if (t >= NS) return;
      if (QT && t == 2 && NS == 3) {
        const unsigned o0 = (sbo + kk * 32 * RS) & w2mask, o1 = (sbo + kk * 32 * RS + 8 * RS) & w2mask;
        wf[t] = tr_frag(smem + wrd[t] + o0, smem + wrd[t] + o1);
      } else if (!QT && t == 3) {
        wf[t] = tr_frag(smem + sbo + wrd[t] + kk * 2048, smem + sbo + wrd[t] + kk * 2048 + 512);
      } else {
        wf[t] = tr_frag(smem + sbo + wrd[t] + kk * 32 * RS, smem + sbo + wrd[t] + kk * 32 * RS + 8 * RS);
      }
    };
    auto rdx = [&](bf16x8(&xf)[4], unsigned sbo, int kk, int i) {
      xf[i] = *(const LAS bf16x8*)(smem + sbo + (kk ? xrd1[i] : xrd0[i]));
    };
    auto compute = [&](bf16x8(&xf)[4], bf16x8(&wf)[NS], bf16x8(&xn)[4], bf16x8(&wn)[NS], unsigned sb, int kk, int op0,
                       int sd, unsigned bufoff) {
#pragma unroll
      for (int t = 0; t < 3; ++t) {
        acc[0][t] = mfma16(wf[t], xf[0], acc[0][t]);
        acc[1][t] = mfma16(wf[t], xf[1], acc[1][t]);
        SB();
        rdw(wn, sb, kk, t);
        if (t < 2) {
          rdx(xn, sb, kk, 2 * t);
          rdx(xn, sb, kk, 2 * t + 1);
        } else {
          rdw(wn, sb, kk, 3);
        }
        SB();
        acc[2][t] = mfma16(wf[t], xf[2], acc[2][t]);
        acc[3][t] = mfma16(wf[t], xf[3], acc[3][t]);
        if (t == 0) gram = mfma16(xf[0], xf[0], gram);
        SB();
        memop(op0 + t, sd, bufoff);
        SB();
      }
      if (NS == 4) {
#pragma unroll
        for (int i = 0; i < 4; ++i) acc[i][3] = mfma16(wf[NS - 1], xf[i], acc[i][3]);
      }
      if (!QT) rsum = mfma16(onesv, xf[0], rsum);
      cm0 = mfma16(wf[MW == 0 ? 0 : 2], onesv, cm0);
      if (MW == 0 || NS == 4) cm1 = mfma16(wf[MW == 0 ? 1 : NS - 1], onesv, cm1);
      SB();
      memop(op0 + 3, sd, bufoff);
      SB();
    };
#pragma unroll
    for (int t = 0; t < 4; ++t) {
      rdw(wf0, 0u, 0, t);
      rdx(xf0, 0u, 0, t);
    }
#pragma unroll
    for (int op = 0; op < 4; ++op) memop(op, 2, 2 * K1_STAGE);
    unsigned bufc = 0, bufo = 2 * K1_STAGE;
#pragma unroll 1
    for (int s = 0; s < 10; ++s) {
      compute(xf0, wf0, xf1, wf1, bufc, 1, 4, s + 2, bufo);
      bufo = bufc;
      bufc = bufc == 2 * K1_STAGE ? 0 : bufc + K1_STAGE;
      __builtin_amdgcn_s_waitcnt(0xC07F);
      asm volatile("s_waitcnt vmcnt(%0)\n\ts_barrier" ::"n"(NDMA + 2) : "memory");
      compute(xf1, wf1, xf0, wf0, bufc, 0, 0, s + 3, bufo);
    }
  };
  if (mw == 0) {
    if (nw < 2) mainloop(IntC<0>{}, IntC<4>{});
    else mainloop(IntC<0>{}, IntC<3>{});
  } else {
    if (nw < 2) mainloop(IntC<1>{}, IntC<4>{});
    else mainloop(IntC<1>{}, IntC<3>{});
  }

  LAS float* st_sum = (LAS float*)(smem + K1_ST);
  LAS float* st_sq = st_sum + 128;
  LAS float* st_cs = st_sum + 256;
  {
    const int r = l15 & 3;
    const float dg = r == 0 ? gram[0] : r == 1 ? gram[1] : r == 2 ? gram[2] : gram[3];
    if (g == (l15 >> 2)) st_sq[(4 * mw + nw) * 16 + l15] = dg;
    if (l15 == 0) {
      *(LAS f32x4*)(st_cs + (mw == 0 ? tsl[0] : tsl[2]) * 16 + 4 * g) = cm0;
      *(LAS f32x4*)(st_cs + (mw == 0 ? tsl[1] : tsl[3]) * 16 + 4 * g) = cm1;
    }
    if (QT) {
      if (g == 0 && nw == 3) {
#pragma unroll
        for (int i = 0; i < 4; ++i) st_sum[(4 * mw + ((i + nw) & 3)) * 16 + l15] = acc[i][2][0];
      }
    } else {
      if (g == 0) st_sum[(4 * mw + nw) * 16 + l15] = rsum[0];
    }
  }
  asm volatile("s_waitcnt vmcnt(0) lgkmcnt(0)\n\ts_barrier" ::: "memory");
  float mr[4], rstd[4], r2[4];
#pragma unroll
  for (int i = 0; i < 4; ++i) {
    const int row = (4 * mw + ((i + nw) & 3)) * 16 + l15;
    const float mean = st_sum[row] * (1.f / 640.f);
    float var = __builtin_fmaf(st_sq[row], 1.f / 640.f, -mean * mean);
    var = var > 0.f ? var : 0.f;
    const float r = __builtin_amdgcn_rsqf(var + 1e-5f);
    r2[i] = QT ? 1.f : __builtin_amdgcn_rsqf(var * r * r + 1e-5f);
    rstd[i] = r;
    mr[i] = -mean * r;
  }
  auto store_pair = [&](int i, int ubase, int Tg, u32x2 a, u32x2 b) {
    const auto s0 = __builtin_amdgcn_permlane16_swap(a[0], b[0], false, false);
    const auto s1 = __builtin_amdgcn_permlane16_swap(a[1], b[1], false, false);
    const u32x4 o = {s0[0], s1[0], s0[1], s1[1]};
    const int m16 = mtile * 8 + mw * 4 + ((i + (g & 1) + nw) & 3);
    const unsigned voff = afrag(m16, 0) + ((g >> 1) * 16 + l15) * 16 + (unsigned)OFF_AF + (ubase + (Tg >> 1)) * 4096 + (Tg & 1) * 512;
    __builtin_amdgcn_raw_buffer_store_b128(o, rws, voff, 0, 16);
  };
  constexpr int PVS = 352;
  auto tile = [&](int t, int dst, bool isphi, bool isq) {
    const f32x4 cs = *(const LAS f32x4*)(st_cs + tsl[t] * 16 + 4 * g);
    u32x2 o[4];
#pragma unroll
    for (int i = 0; i < 4; ++i) {
      float v[4];
#pragma unroll
      for (int r = 0; r < 4; ++r) {
        if (isphi) {
          const float tp = __builtin_fmaf(acc[i][t][r], rstd[i], __builtin_fmaf(mr[i], cs[r], 1e-6f));
          const float e = __builtin_amdgcn_exp2f(__builtin_fmaf(tp, 1.4426950408889634f, -1.4426950408889634e-6f));
          v[r] = __builtin_amdgcn_fmed3f(e, 0.f, 1.f) + __builtin_fmaxf(tp, 1e-6f);
        } else {
          v[r] = __builtin_fmaf(acc[i][t][r], rstd[i], mr[i] * cs[r]);
        }
      }
      o[i] = (u32x2){pack2(v[0], v[1]), pack2(v[2], v[3])};
      if (isq) {
        if (i & 1) store_pair(i - 1, 48, grp * 5 + dst, o[i - 1], o[i]);
      } else {
        *(LAS u32x2*)(smem + (mw * 64 + ((i + nw) & 3) * 16 + l15) * PVS + dst * 32 + g * 8) = o[i];
      }
    }
  };

  if (!QT) {
    if (nw < 2) tile(3, 3 + nw, true, true);
#pragma unroll
    for (int i = 0; i < 4; ++i) {
      rstd[i] *= r2[i];
      mr[i] *= r2[i];
    }
#pragma unroll
    for (int t = 0; t < 3; ++t) {
      const int Tg = grp * 12 + tsl[t];
      const f32x4 cs = *(const LAS f32x4*)(st_cs + tsl[t] * 16 + 4 * g);
      const float bv[4] = {bflo(b1r[t][0]), bfhi(b1r[t][0]), bflo(b1r[t][1]), bfhi(b1r[t][1])};
      u32x2 o[4];
#pragma unroll
      for (int i = 0; i < 4; ++i) {
        float hv[4];
#pragma unroll
        for (int r = 0; r < 4; ++r)
          hv[r] = gelu_tanh(__builtin_fmaf(acc[i][t][r], rstd[i], __builtin_fmaf(mr[i], cs[r], bv[r])));
        o[i] = (u32x2){pack2(hv[0], hv[1]), pack2(hv[2], hv[3])};
        if (i & 1) store_pair(i - 1, 0, Tg, o[i - 1], o[i]);
      }
    }
    if (job < 960) {
      const int u = job / 20, nb = job - u * 20;
      u32x4 lo, hi;
#pragma unroll
      for (int j = 0; j < 4; ++j) {
        lo[j] = __builtin_amdgcn_perm(w2r[2 * j + 1], w2r[2 * j], 0x05040100u);
        hi[j] = __builtin_amdgcn_perm(w2r[2 * j + 1], w2r[2 * j], 0x07060302u);
      }
      const int n16 = nb * 2 + (l15 >> 3), nl = (2 * l15) & 15;
      const unsigned dst = (unsigned)OFF_BF + bfrag(n16, u) + (g * 16 + nl) * 16;
      __builtin_amdgcn_raw_buffer_store_b128(lo, rws, dst, 0, 0);
      __builtin_amdgcn_raw_buffer_store_b128(hi, rws, dst + 16, 0, 0);
    }
  } else {
    if (tid < 400) {
      const int c = (mtile * 8 + grp) * 400 + tid, n80 = c / 6400, r = c - n80 * 6400;
      __builtin_amdgcn_raw_buffer_store_b128((u32x4){~0u, ~0u, ~0u, ~0u}, rws,
                                             (unsigned)OFF_BF + (unsigned)((n80 * NU + 48) * 5120 + r * 16), 0, 16);
    }
    if (nw < 3) tile(2, nw, true, true);
    tile(0, nw, true, false);
    tile(1, 5 + nw, false, false);
    if (nw == 0) tile(3, 4, true, false);
    if (nw == 1) tile(3, 9, false, false);
    asm volatile("s_waitcnt lgkmcnt(0)\n\ts_barrier" ::: "memory");
    if (wid < 5) {
      const int dt = wid;
      const int trow = (4 * g + dq) * PVS + (p >> 1) * 16 + (p & 1) * 8;
      f32x4 kvacc[3], zacc = {0.f, 0.f, 0.f, 0.f};
#pragma unroll
      for (int j = 0; j < 3; ++j) kvacc[j] = (f32x4){0.f, 0.f, 0.f, 0.f};
#pragma unroll
      for (int ks = 0; ks < 4; ++ks) {
        const LAS char* pb = smem + ks * 32 * PVS + trow;
        const bf16x8 a = tr_frag(pb + dt * 32, pb + dt * 32 + 16 * PVS);
        zacc = mfma16(a, onesv, zacc);
#pragma unroll
        for (int j = 0; j < 3; ++j) {
          int et = dt - 1 + j;
          et = et < 0 ? 0 : (et > 4 ? 4 : et);
          const bf16x8 b = tr_frag(pb + 160 + et * 32, pb + 160 + et * 32 + 16 * PVS);
          kvacc[j] = mfma16(a, b, kvacc[j]);
        }
      }
      const int dl = dt * 16 + 4 * g, hd = dl / 20, dg = (dl - hd * 20) >> 2;
      const unsigned kvp = (unsigned)OFF_KVP + (unsigned)(((grp * 4 + hd) * 16 + mtile) * 1600);
#pragma unroll
      for (int j = 0; j < 3; ++j) {
        const int e = (dt - 1 + j) * 16 + l15 - hd * 20;
        if (e >= 0 && e < 20)
          __builtin_amdgcn_raw_buffer_store_b128(__builtin_bit_cast(u32x4, kvacc[j]), rws, kvp + (dg * 20 + e) * 16, 0, 0);
      }
      if (l15 == 0)
        __builtin_amdgcn_raw_buffer_store_b128(__builtin_bit_cast(u32x4, zacc), rws,
                                               (unsigned)OFF_ZP + (mtile * 640 + grp * 80 + dl) * 4, 0, 0);
    }
  }
}

__global__ void __launch_bounds__(512, 2)
k1_kernel(const __hip_bfloat16* __restrict__ x, const __hip_bfloat16* __restrict__ wqkv,
          const __hip_bfloat16* __restrict__ w1, const __hip_bfloat16* __restrict__ b1,
          const __hip_bfloat16* __restrict__ w2, char* __restrict__ ws) {
  extern __shared__ __attribute__((aligned(16))) char smem_[];
  LAS char* smem = (LAS char*)smem_;
  const int b = blockIdx.x, xcd = b & 7, idx = b >> 3;
  const int mtile = (xcd & 1) * 8 + (idx & 7), sel = idx >> 3;
  const int grp = (xcd >> 1) * 2 + (sel & 1);
  if (idx >> 4)
    k1_body<false>(x, wqkv, w1, b1, w2, ws, mtile, grp, sel, smem);
  else
    k1_body<true>(x, wqkv, w1, b1, w2, ws, mtile, grp, sel, smem);
}

constexpr int K2_LDS = 163840;
constexpr int K2_RED = 1024;
constexpr int K2_KVT = 4 * 8192;
constexpr int K2_WOS = 5 * 8192;
constexpr int K2_ZB = 6 * 8192;
constexpr int K2_CNT = 0;

__device__ __forceinline__ unsigned helper_arrive(LAS unsigned* c, int lane) {
  unsigned old = 0;
  asm volatile("" ::: "memory");
  __builtin_amdgcn_s_waitcnt(0xC07F);
  if (lane == 0) old = __hip_atomic_fetch_add(c, 1u, __ATOMIC_RELAXED, __HIP_MEMORY_SCOPE_WORKGROUP);
  return __builtin_amdgcn_readfirstlane(old);
}
__device__ __forceinline__ void helper_wait(LAS unsigned* c) {
  while (__builtin_amdgcn_readfirstlane(__hip_atomic_load(c, __ATOMIC_RELAXED, __HIP_MEMORY_SCOPE_WORKGROUP)) < 4u)
    __builtin_amdgcn_s_sleep(1);
  asm volatile("" ::: "memory");
}

__global__ void __launch_bounds__(512, 2)
k2_kernel(const __hip_bfloat16* __restrict__ x, const __hip_bfloat16* __restrict__ wo,
          const __hip_bfloat16* __restrict__ b2, char* __restrict__ ws, __hip_bfloat16* __restrict__ out) {
  extern __shared__ __attribute__((aligned(16))) char smem_[];
  LAS char* smem = (LAS char*)smem_;
  const int tid = threadIdx.x, lane = tid & 63;
  const int wid = __builtin_amdgcn_readfirstlane(tid >> 6);
  const int w = wid & 3, helper = wid >> 2;
  const int l15 = lane & 15, g = lane >> 4;
  const int b = blockIdx.x, xcd = b & 7, idx = b >> 3;
  const int mt = (xcd & 3) * 8 + (idx & 7);
  const int nt = (xcd >> 2) * 4 + (idx >> 3);
  const int ph = xcd * 4 + (idx & 3), ps = idx >> 2;

  const __amdgpu_buffer_rsrc_t rab = __builtin_amdgcn_make_buffer_rsrc(ws + OFF_AF, 0, 168 * NU * 1024, 0x00020000);
  const unsigned aoff = (unsigned)(mt * 4 * NU * 1024 + lane * 16);
  const unsigned boff = (unsigned)((128 + nt * 5) * NU * 1024 + lane * 16);
  const int u0 = w * 12, uo = 48 + w * 5;
  LAS unsigned* cnt = (LAS unsigned*)(smem + K2_CNT);
  const int token = mt * 64 + w * 16 + l15;
  u32x2 xr[3], br[3];
  auto load_resid = [&]() {
#pragma unroll
    for (int j = 0; j < 3; ++j) {
      const int jj = helper * 3 + j < 5 ? helper * 3 + j : 4;
      xr[j] = *(const u32x2*)(x + (size_t)token * D_ + nt * 80 + jj * 16 + 4 * g);
      br[j] = *(const u32x2*)(b2 + nt * 80 + jj * 16 + 4 * g);
    }
  };

  f32x4 own[3];
#pragma unroll
  for (int j = 0; j < 3; ++j) own[j] = (f32x4){0.f, 0.f, 0.f, 0.f};

  if (helper == 0) {
    f32x4 acc[4][5];
#pragma unroll
    for (int i = 0; i < 4; ++i)
#pragma unroll
      for (int j = 0; j < 5; ++j) acc[i][j] = (f32x4){0.f, 0.f, 0.f, 0.f};
    u32x4 fa[4][4], fb[4][5];
    auto load_one = [&](int s, int n) {
      const int slot = s & 3;
      if (s < 12) {
        if (n < 5)
          fb[slot][n] = __builtin_amdgcn_raw_buffer_load_b128(rab, boff + n * 1024, (u0 + s) * 5120, 0);
        else
          fa[slot][n - 5] = __builtin_amdgcn_raw_buffer_load_b128(rab, aoff + (n - 5) * 1024, (u0 + s) * 4096, 0);
      }
    };
    asm volatile("s_barrier" ::: "memory");
#pragma unroll
    for (int k = 0; k < 3; ++k)
#pragma unroll
      for (int n = 0; n < 9; ++n) load_one(k, n);
    SB();
    auto step = [&](auto sc_) {
      constexpr int s = decltype(sc_)::value;
      constexpr int slot = s & 3;
#pragma unroll
      for (int q = 0; q < 10; ++q) {
#pragma unroll
        for (int h2 = 0; h2 < 2; ++h2) {
          const int i = (2 * q + h2) / 5, j = (2 * q + h2) % 5;
          acc[i][j] = mfma16(__builtin_bit_cast(bf16x8, fb[slot][j]), __builtin_bit_cast(bf16x8, fa[slot][i]), acc[i][j]);
        }
        SB();
        if (q < 9) load_one(s + 3, q);
        SB();
      }
      if constexpr (s == 8) load_resid();
    };
    static_for<0, 12>(step);
    SB();
    helper_wait(cnt + 2);
#pragma unroll
    for (int i = 0; i < 4; ++i) {
      if (i == w) {
        asm volatile("" ::: "memory");
#pragma unroll
        for (int j = 0; j < 3; ++j) own[j] = acc[i][j];
      } else {
#pragma unroll
        for (int j = 0; j < 3; ++j) *(LAS f32x4*)(smem + (((i * 5 + j) * 8 + wid) * 64 + lane) * 16) = acc[i][j];
      }
#pragma unroll
      for (int j = 3; j < 5; ++j) *(LAS f32x4*)(smem + (((i * 5 + j) * 8 + wid) * 64 + lane) * 16) = acc[i][j];
    }
  } else {
    const int ht = tid - 256;
    __builtin_amdgcn_s_setprio(3);
    LAS float* kvt = (LAS float*)(smem + K2_KVT);
    LAS char* wos = smem + K2_WOS;
    f32x4 kvl[4][2];
    {
      const char* src = ws + OFF_KVP + (size_t)(ph * 16 + w * 4) * 1600;
#pragma unroll
      for (int pidx = 0; pidx < 4; ++pidx) {
        kvl[pidx][0] = *(const f32x4*)(src + pidx * 1600 + lane * 16);
        kvl[pidx][1] = *(const f32x4*)(src + pidx * 1600 + (lane < 36 ? lane + 64 : 99) * 16);
      }
    }
    const int tc = ht < 200 ? ht : 199;
    const int pg = tc / 40, pn = tc - pg * 40;
    const u32x4 wol = *(const u32x4*)(wo + (size_t)(ph * 20 + tc / 10) * D_ + ps * 80 + (tc % 10) * 8);
    if (ht < 4) cnt[ht] = 0u;
    SB();
    asm volatile("s_waitcnt lgkmcnt(0)\n\ts_barrier" ::: "memory");
    *(LAS f32x4*)(smem + K2_RED + w * 8192 + lane * 16) = (kvl[0][0] + kvl[1][0]) + (kvl[2][0] + kvl[3][0]);
    *(LAS f32x4*)(smem + K2_RED + w * 8192 + (64 + lane) * 16) = (kvl[0][1] + kvl[1][1]) + (kvl[2][1] + kvl[3][1]);
    *(LAS u32x4*)(wos + tc * 16) = wol;
    helper_arrive(cnt, lane);
    helper_wait(cnt);
    if (w < 2) {
      const LAS char* r = smem + K2_RED + ht * 16;
      const f32x4 t = (*(const LAS f32x4*)r + *(const LAS f32x4*)(r + 8192)) +
                      (*(const LAS f32x4*)(r + 16384) + *(const LAS f32x4*)(r + 24576));
      const u32x2 pk = {pack2(t[0], t[1]), pack2(t[2], t[3])};
      const int idg = ht / 20, ie = ht - idg * 20;
      if (ht < 100) *(LAS f32x4*)(kvt + ie * 20 + idg * 4) = (f32x4){bflo(pk[0]), bfhi(pk[0]), bflo(pk[1]), bfhi(pk[1])};
    }
    helper_arrive(cnt + 1, lane);
    helper_wait(cnt + 1);
    {
      f32x4 o0 = {0.f, 0.f, 0.f, 0.f}, o1 = {0.f, 0.f, 0.f, 0.f};
#pragma unroll
      for (int e = 0; e < 20; ++e) {
        const f32x4 kq = *(const LAS f32x4*)(kvt + e * 20 + pg * 4);
        const unsigned wv = *(const LAS unsigned*)(wos + e * 160 + pn * 4);
        o0 += kq * bflo(wv);
        o1 += kq * bfhi(wv);
      }
      const int q = 5 * ph + pg, n = ps * 80 + 2 * pn;
      char* dst = ws + OFF_BF + bfrag(n >> 4, 48 + (q >> 3)) + (((q >> 1) & 3) * 16 + (n & 15)) * 16 + (q & 1) * 8;
      const u32x2 v0 = {pack2(o0[0], o0[1]), pack2(o0[2], o0[3])}, v1 = {pack2(o1[0], o1[1]), pack2(o1[2], o1[3])};
      asm volatile("global_store_dwordx2 %0, %1, off sc1\n\tglobal_store_dwordx2 %0, %2, off offset:16 sc1"
                   ::"v"(dst), "v"(v0), "v"(v1) : "memory");
    }
    f32x4 zl[16];
    {
      const char* src = ws + OFF_ZP + (w * 40 + (lane < 40 ? lane : 39)) * 16;
#pragma unroll
      for (int pidx = 0; pidx < 16; ++pidx) zl[pidx] = *(const f32x4*)(src + pidx * 2560);
    }
    u32x4 pq[5][4];
#pragma unroll
    for (int ul = 0; ul < 5; ++ul)
#pragma unroll
      for (int i = 0; i < 4; ++i)
        pq[ul][i] = __builtin_amdgcn_raw_buffer_load_b128(rab, aoff + i * 1024, (uo + ul) * 4096, 0);
    SB();
    {
      f32x4 zs = zl[0];
#pragma unroll
      for (int pidx = 1; pidx < 16; ++pidx) zs += zl[pidx];
      if (lane < 40) *(LAS u32x2*)(smem + K2_ZB + w * 320 + lane * 8) = (u32x2){pack2(zs[0], zs[1]), pack2(zs[2], zs[3])};
    }
    u32x4 zp[5];
#pragma unroll
    for (int ul = 0; ul < 5; ++ul) zp[ul] = *(const LAS u32x4*)(smem + K2_ZB + w * 320 + (32 * ul + 8 * g) * 2);
    helper_arrive(cnt + 2, lane);
    u32x4 fb[3][5];
    auto load_wp = [&](int ul) {
#pragma unroll
      for (int j = 0; j < 5; ++j)
        fb[ul % 3][j] = __builtin_amdgcn_raw_buffer_load_b128(rab, boff + j * 1024, (uo + ul) * 5120, 16);
    };
    load_wp(0);
    load_wp(1);
    load_wp(2);
    SB();
#pragma unroll
    for (int ih = 0; ih < 2; ++ih) {
      float rden[2][12];
#pragma unroll
      for (int c = 0; c < 3; ++c) {
        const int tau = 4 * c + (l15 & 3);
        const int htar = (32 * (tau >> 1) + 8 * (l15 >> 2) + 4 * (tau & 1)) / 20;
        f32x4 dacc[2];
#pragma unroll
        for (int i = 0; i < 2; ++i) dacc[i] = (f32x4){1e-6f, 1e-6f, 1e-6f, 1e-6f};
#pragma unroll
        for (int ul = 0; ul < 5; ++ul) {
          if (c == 0 && ul > 2) continue;
          if (c == 1 && ul < 1) continue;
          if (c == 2 && ul < 3) continue;
          u32x4 zf;
#pragma unroll
          for (int i = 0; i < 2; ++i) {
            const bool m = (32 * ul + 8 * g + 4 * i) / 20 == htar;
            zf[2 * i] = m ? zp[ul][2 * i] : 0u;
            zf[2 * i + 1] = m ? zp[ul][2 * i + 1] : 0u;
          }
#pragma unroll
          for (int i = 0; i < 2; ++i)
            dacc[i] = mfma16(__builtin_bit_cast(bf16x8, zf), __builtin_bit_cast(bf16x8, pq[ul][2 * ih + i]), dacc[i]);
        }
#pragma unroll
        for (int i = 0; i < 2; ++i)
#pragma unroll
          for (int r = 0; r < 4; ++r)
            if (4 * c + r < 10) rden[i][4 * c + r] = __builtin_amdgcn_rcpf(dacc[i][r]);
      }
#pragma unroll
      for (int ul = 0; ul < 5; ++ul)
#pragma unroll
        for (int i = 0; i < 2; ++i) {
#pragma unroll
          for (int j = 0; j < 4; ++j) {
            const float sc = rden[i][2 * ul + (j >> 1)];
            pq[ul][2 * ih + i][j] = pack2(bflo(pq[ul][2 * ih + i][j]) * sc, bfhi(pq[ul][2 * ih + i][j]) * sc);
          }
          asm volatile("" : "+v"(pq[ul][2 * ih + i]));
        }
      SB();
    }
    f32x4 acc[4][5];
#pragma unroll
    for (int i = 0; i < 4; ++i)
#pragma unroll
      for (int j = 0; j < 5; ++j) acc[i][j] = (f32x4){0.f, 0.f, 0.f, 0.f};
    auto ostep = [&](auto sc_) {
      constexpr int ul = decltype(sc_)::value;
      auto missing = [&]() {
        unsigned m = 0;
#pragma unroll
        for (int j = 0; j < 5; ++j) m |= (fb[ul % 3][j][0] == ~0u) | (fb[ul % 3][j][2] == ~0u);
        return __any(m);
      };
      if (__builtin_expect(missing(), 0)) {
        unsigned spins = 0;
        do {
          __builtin_amdgcn_s_sleep(2);
          load_wp(ul);
        } while (missing() && ++spins < (1u << 18));
      }
#pragma unroll
      for (int i = 0; i < 4; ++i)
#pragma unroll
        for (int j = 0; j < 5; ++j)
          acc[i][j] = mfma16(__builtin_bit_cast(bf16x8, fb[ul % 3][j]), __builtin_bit_cast(bf16x8, pq[ul][i]), acc[i][j]);
      SB();
      if constexpr (ul + 3 < 5) load_wp(ul + 3);
      if constexpr (ul == 2) load_resid();
      SB();
    };
    static_for<0, 5>(ostep);
#pragma unroll
    for (int i = 0; i < 4; ++i)
#pragma unroll
      for (int j = 0; j < 5; ++j) *(LAS f32x4*)(smem + (((i * 5 + j) * 8 + wid) * 64 + lane) * 16) = acc[i][j];
  }

  asm volatile("s_waitcnt lgkmcnt(0)\n\ts_barrier" ::: "memory");
#pragma unroll
  for (int j = 0; j < 3; ++j) {
    if (j == 2 && helper) break;
    const int jj = helper * 3 + j;
    f32x4 fin = own[j];
#pragma unroll
    for (int sidx = 1; sidx < 8; ++sidx) fin += *(const LAS f32x4*)(smem + (((w * 5 + jj) * 8 + ((w + sidx) & 7)) * 64 + lane) * 16);
    if (helper) fin += *(const LAS f32x4*)(smem + (((w * 5 + jj) * 8 + w) * 64 + lane) * 16);
    const float o0 = fin[0] + bflo(xr[j][0]) + bflo(br[j][0]);
    const float o1 = fin[1] + bfhi(xr[j][0]) + bfhi(br[j][0]);
    const float o2 = fin[2] + bflo(xr[j][1]) + bflo(br[j][1]);
    const float o3 = fin[3] + bfhi(xr[j][1]) + bfhi(br[j][1]);
    *(u32x2*)(out + (size_t)token * D_ + nt * 80 + jj * 16 + 4 * g) = (u32x2){pack2(o0, o1), pack2(o2, o3)};
  }
}

}

extern "C" void kernel_launch(void* const* d_in, const int* in_sizes, int n_in, void* d_out, int out_size, void* d_ws,
                              size_t ws_size, hipStream_t stream) {
  const __hip_bfloat16* x = (const __hip_bfloat16*)d_in[0];
  const __hip_bfloat16* wqkv = (const __hip_bfloat16*)d_in[1];
  const __hip_bfloat16* wo = (const __hip_bfloat16*)d_in[2];
  const __hip_bfloat16* w1 = (const __hip_bfloat16*)d_in[4];
  const __hip_bfloat16* b1 = (const __hip_bfloat16*)d_in[5];
  const __hip_bfloat16* w2 = (const __hip_bfloat16*)d_in[6];
  const __hip_bfloat16* b2 = (const __hip_bfloat16*)d_in[7];
  (void)hipFuncSetAttribute((const void*)k1_kernel, hipFuncAttributeMaxDynamicSharedMemorySize, K1_LDS);
  (void)hipFuncSetAttribute((const void*)k2_kernel, hipFuncAttributeMaxDynamicSharedMemorySize, K2_LDS);
  k1_kernel<<<256, 512, K1_LDS, stream>>>(x, wqkv, w1, b1, w2, (char*)d_ws);
  k2_kernel<<<256, 512, K2_LDS, stream>>>(x, wo, b2, (char*)d_ws, (__hip_bfloat16*)d_out);
}
```
